```python
import torch

def _softmax(x, axis=-1):
    return torch.softmax(x, dim=axis)
def _gelu(x):
    return torch.nn.functional.gelu(x, approximate="tanh")
def _silu(x):
    return torch.nn.functional.silu(x)
def _sigmoid(x):
    return torch.sigmoid(x)
def _layernorm(x, axis=-1, eps=1e-5):
    m = x.mean(dim=axis, keepdim=True)
    v = x.var(dim=axis, keepdim=True, unbiased=False)
    return (x - m) / torch.sqrt(v + eps)
def _scatter_add(tgt, idx, src, axis=0):
    return tgt.clone().index_add_(axis, idx, src)
def _segment_sum(src, seg, n):
    out = torch.zeros((n,) + src.shape[1:], dtype=src.dtype)
    return out.index_add_(0, seg, src)
def _pad(x, pw):
    flat = tuple(v for pair in reversed(pw) for v in pair)
    return torch.nn.functional.pad(x, flat)

kernel_name = 'interleave_layers__mha_block__glu_gated_reglu__prompt'

def setup_inputs():

    _w = (lambda a: (lambda t: t.to(torch.bfloat16) if t.is_floating_point() else t)(torch.as_tensor(a)))
    def _A_setup():
        B, S, D, H = 1, 2048, 640, 32
        return {
            "x": _w((lambda *s: torch.randn(*s, dtype=torch.bfloat16) * 0.05)(B, S, D)),
            "wqkv": _w((lambda *s: torch.randn(*s, dtype=torch.bfloat16) * 0.05)(D, 3 * D)),
            "wo": _w((lambda *s: torch.randn(*s, dtype=torch.bfloat16) * 0.05)(D, D)),
        }
    _A_in = _A_setup()

    def _B_setup():
        B, S, D, F = 1, 2048, 640, 1728
        return {
            "x": _w((lambda *s: torch.randn(*s, dtype=torch.bfloat16) * 0.05)(B, S, D)),
            "W_gate": _w((lambda *s: torch.randn(*s, dtype=torch.bfloat16) * 0.8)(D, F) * (2.0 / D) ** 0.5),
            "W_up": _w((lambda *s: torch.randn(*s, dtype=torch.bfloat16) * 0.8)(D, F) * (2.0 / D) ** 0.5),
            "W_down": _w((lambda *s: torch.randn(*s, dtype=torch.bfloat16) * 0.8)(F, D) * (1.0 / F) ** 0.5),
        }
    _B_in = _B_setup()

    return {
        "A_x": _A_in["x"],
        "A_wqkv": _A_in["wqkv"],
        "A_wo": _A_in["wo"],
        "B_x": _B_in["x"],
        "B_W_gate": _B_in["W_gate"],
        "B_W_up": _B_in["W_up"],
        "B_W_down": _B_in["W_down"],
    }

def reference(A_x, A_wqkv, A_wo, B_x, B_W_gate, B_W_up, B_W_down):
    def _A_ref(x, wqkv, wo):
        B, S, D = x.shape
        H = 32
        Dh = D // H
        qkv = (lambda x, *d: x.permute(*d))((x @ wqkv).reshape(B, S, 3, H, Dh), 2, 0, 3, 1, 4)
        q, k, v = qkv[0], qkv[1], qkv[2]
        scores = torch.einsum("bhqd,bhkd->bhqk", q, k) * (Dh ** -0.5)
        p = _softmax(scores, axis=-1)
        o = torch.einsum("bhqk,bhkd->bhqd", p, v)
        return (lambda x, *d: x.permute(*d))(o, 0, 2, 1, 3).reshape(B, S, D) @ wo

    def _B_ref(x, W_gate, W_up, W_down):
        gate = torch.einsum("bsd,df->bsf", x, W_gate)
        up = torch.einsum("bsd,df->bsf", x, W_up)
        gate = gate * (lambda x, dt: x.to(dt))(gate > 0, gate.dtype)
        h = gate * up
        return torch.einsum("bsf,fd->bsd", h, W_down)

    x = A_x
    x = x + _A_ref(_layernorm(x, axis=-1), A_wqkv, A_wo)
    x = x + _B_ref(_layernorm(x, axis=-1), B_W_gate, B_W_up, B_W_down)
    x = x + _A_ref(_layernorm(x, axis=-1), A_wqkv, A_wo)
    x = x + _B_ref(_layernorm(x, axis=-1), B_W_gate, B_W_up, B_W_down)
    return x
```

```cpp
#include <hip/hip_runtime.h>
#include <hip/hip_bf16.h>
typedef unsigned short u16;
__device__ __forceinline__ float b2f(u16 b) { return __uint_as_float((unsigned)b << 16); }
__device__ __forceinline__ u16 f2b(float f) { unsigned u = __float_as_uint(f); u += 0x7FFFu + ((u >> 16) & 1u); return (u16)(u >> 16); }
constexpr int S = 2048, D = 640, H = 32, DH = 20, F = 1728;
__global__ void ln_k(const u16* x, u16* y) {
  int row = blockIdx.x; int t = threadIdx.x;
  float v[10]; float s = 0;
  for (int i = 0; i < 10; ++i) { v[i] = b2f(x[row * D + t + 64 * i]); s += v[i]; }
  for (int o = 32; o; o >>= 1) s += __shfl_xor(s, o);
  float m = s / D; float q = 0;
  for (int i = 0; i < 10; ++i) { float d = v[i] - m; q += d * d; }
  for (int o = 32; o; o >>= 1) q += __shfl_xor(q, o);
  float r = rsqrtf(q / D + 1e-5f);
  for (int i = 0; i < 10; ++i) y[row * D + t + 64 * i] = f2b((v[i] - m) * r);
}
template<int MODE>
__global__ void gemm_k(const u16* A, const u16* B, const u16* B2, const u16* res, u16* C, int M, int N, int K) {
  __shared__ float sa[16][17], sb[16][17], sb2[16][17];
  int tx = threadIdx.x & 15, ty = threadIdx.x >> 4;
  int row = blockIdx.y * 16 + ty, col = blockIdx.x * 16 + tx;
  float acc = 0, acc2 = 0;
  for (int k0 = 0; k0 < K; k0 += 16) {
    sa[ty][tx] = b2f(A[row * K + k0 + tx]);
    sb[ty][tx] = b2f(B[(k0 + ty) * N + col]);
    if (MODE == 1) sb2[ty][tx] = b2f(B2[(k0 + ty) * N + col]);
    __syncthreads();
    for (int k = 0; k < 16; ++k) { acc += sa[ty][k] * sb[k][tx]; if (MODE == 1) acc2 += sa[ty][k] * sb2[k][tx]; }
    __syncthreads();
  }
  if (MODE == 1) acc = (acc > 0 ? acc : 0) * acc2;
  if (MODE == 2) acc += b2f(res[row * N + col]);
  C[row * N + col] = f2b(acc);
}
__global__ void attn_k(const u16* qkv, u16* o) {
  int h = blockIdx.y; int q = blockIdx.x * 64 + threadIdx.x;
  __shared__ float sk[64][DH], sv[64][DH];
  float qq[DH], acc[DH]; for (int d = 0; d < DH; ++d) { qq[d] = b2f(qkv[q * 3 * D + h * DH + d]) * 0.22360679775f; acc[d] = 0; }
  float m = -1e30f, l = 0;
  for (int k0 = 0; k0 < S; k0 += 64) {
    __syncthreads();
    for (int d = 0; d < DH; ++d) { sk[threadIdx.x][d] = b2f(qkv[(k0 + threadIdx.x) * 3 * D + D + h * DH + d]); sv[threadIdx.x][d] = b2f(qkv[(k0 + threadIdx.x) * 3 * D + 2 * D + h * DH + d]); }
    __syncthreads();
    for (int k = 0; k < 64; ++k) {
      float s = 0; for (int d = 0; d < DH; ++d) s += qq[d] * sk[k][d];
      float mn = fmaxf(m, s); float a = __expf(m - mn), p = __expf(s - mn);
      l = l * a + p; for (int d = 0; d < DH; ++d) acc[d] = acc[d] * a + p * sv[k][d];
      m = mn;
    }
  }
  for (int d = 0; d < DH; ++d) o[q * D + h * DH + d] = f2b(acc[d] / l);
}
extern "C" void kernel_launch(void* const* d_in, const int* in_sizes, int n_in, void* d_out, int out_size, void* d_ws, size_t ws_size, hipStream_t stream) {
  const u16* x = (const u16*)d_in[0]; const u16* wqkv = (const u16*)d_in[1]; const u16* wo = (const u16*)d_in[2];
  const u16* wg = (const u16*)d_in[4]; const u16* wu = (const u16*)d_in[5]; const u16* wd = (const u16*)d_in[6];
  u16* ws = (u16*)d_ws; u16* xn = ws; u16* qkv = ws + (1 << 21); u16* o = ws + (4 << 21); u16* x2 = ws + (5 << 21); u16* hh = ws + (6 << 21); u16* x3 = ws + (9 << 21);
  const u16* cur = x;
  for (int l = 0; l < 2; ++l) {
    ln_k<<<S, 64, 0, stream>>>(cur, xn);
    gemm_k<0><<<dim3(3 * D / 16, S / 16), 256, 0, stream>>>(xn, wqkv, nullptr, nullptr, qkv, S, 3 * D, D);
    attn_k<<<dim3(S / 64, H), 64, 0, stream>>>(qkv, o);
    gemm_k<2><<<dim3(D / 16, S / 16), 256, 0, stream>>>(o, wo, nullptr, cur, x2, S, D, D);
    ln_k<<<S, 64, 0, stream>>>(x2, xn);
    gemm_k<1><<<dim3(F / 16, S / 16), 256, 0, stream>>>(xn, wg, wu, nullptr, hh, S, F, D);
    u16* dst = l == 0 ? x3 : (u16*)d_out;
    gemm_k<2><<<dim3(D / 16, S / 16), 256, 0, stream>>>(hh, wd, nullptr, x2, dst, S, D, F);
    cur = x3;
  }
}
```

```cpp
#include <hip/hip_runtime.h>
#include <hip/hip_bf16.h>

typedef unsigned u32;
typedef unsigned long long u64;
typedef u32 u32x2 __attribute__((ext_vector_type(2)));
typedef u32 u32x4 __attribute__((ext_vector_type(4)));
typedef float f32x2 __attribute__((ext_vector_type(2)));
typedef float f32x4 __attribute__((ext_vector_type(4)));
typedef float f32x16 __attribute__((ext_vector_type(16)));
typedef __bf16 bf2 __attribute__((ext_vector_type(2)));
typedef __bf16 bf8 __attribute__((ext_vector_type(8)));
typedef short s16x4 __attribute__((ext_vector_type(4)));
typedef __amdgpu_buffer_rsrc_t rsrc_t;
#define GAS __attribute__((address_space(1)))
#define LAS __attribute__((address_space(3)))
#define DEV __device__ __forceinline__
#define SBAR() __builtin_amdgcn_sched_barrier(0)
#define PIN(x) asm volatile("" : "+v"(x))
#define SPIN(x) asm volatile("" : "+s"(x))
#define FRESH() do { PIN(lane); SPIN(r); SPIN(c); SPIN(L); } while (0)
#define RLX_AGENT __ATOMIC_RELAXED, __HIP_MEMORY_SCOPE_AGENT

constexpr int SEQ = 2048, DM = 640, FF = 1728;
constexpr int SC1 = 16;
constexpr u32 XT_BYTES = 64 * 40 * 1024;
constexpr u32 ST_BYTES = SEQ * 64;
constexpr u32 REC = 2688;
constexpr u32 KV_BYTES = 32 * 64 * REC;
constexpr u32 KM_BYTES = 32 * 64 * 4;
constexpr u32 OT_BYTES = 128 * 20 * 1024;
constexpr u32 HT_BYTES = 128 * 54 * 1024;
constexpr u32 CTRL_BYTES = 65536;
constexpr u32 OFF_XT = CTRL_BYTES;
constexpr u32 OFF_ST = OFF_XT + 3 * XT_BYTES;
constexpr u32 OFF_KV = OFF_ST + 3 * ST_BYTES;
constexpr u32 OFF_KM = OFF_KV + 2 * KV_BYTES;
constexpr u32 OFF_OT = OFF_KM + 2 * KM_BYTES;
constexpr u32 OFF_HT = OFF_OT + 2 * OT_BYTES;
constexpr u32 WS_USED = OFF_HT + 2 * HT_BYTES;
constexpr u32 L_SCR = 143360, L_GAP = 102400, L_BYTES = 163840;

struct Params {
  const unsigned short *x, *wqkv, *wo, *wg, *wu, *wd;
  unsigned short *out;
  unsigned char *ws;
};

DEV float asf(u32 u) { return __builtin_bit_cast(float, u); }
DEV u32 asu(float f) { return __builtin_bit_cast(u32, f); }
DEV u32 pk(float lo, float hi) { f32x2 v = {lo, hi}; return __builtin_bit_cast(u32, __builtin_convertvector(v, bf2)); }
DEV f32x16 mfma32(u32x4 a, u32x4 b, f32x16 c) { return __builtin_amdgcn_mfma_f32_32x32x16_bf16(__builtin_bit_cast(bf8, a), __builtin_bit_cast(bf8, b), c, 0, 0, 0); }
DEV f32x4 mfma16(u32x4 a, u32x4 b, f32x4 c) { return __builtin_amdgcn_mfma_f32_16x16x32_bf16(__builtin_bit_cast(bf8, a), __builtin_bit_cast(bf8, b), c, 0, 0, 0); }
DEV u32x2 ldtr(u32 addr) { return __builtin_bit_cast(u32x2, __builtin_amdgcn_ds_read_tr16_b64_v4i16((LAS s16x4 *)(uintptr_t)addr)); }
DEV float dot2(u32 a, u32 b, float c) { return __builtin_amdgcn_fdot2_f32_bf16(__builtin_bit_cast(bf2, a), __builtin_bit_cast(bf2, b), c, false); }
DEV rsrc_t mkrs(const void *p, u32 bytes) { return __builtin_amdgcn_make_buffer_rsrc((void *)p, 0, bytes, 0x00020000); }
template <int AUX> DEV u32x4 ld128(rsrc_t r, u32 v, u32 s) { return __builtin_amdgcn_raw_buffer_load_b128(r, v, s, AUX); }
template <int AUX> DEV u32x2 ld64(rsrc_t r, u32 v, u32 s) { return __builtin_amdgcn_raw_buffer_load_b64(r, v, s, AUX); }
template <int AUX> DEV void st128(u32x4 d, rsrc_t r, u32 v, u32 s) { __builtin_amdgcn_raw_buffer_store_b128(d, r, v, s, AUX); }
template <int AUX> DEV void st64(u32x2 d, rsrc_t r, u32 v, u32 s) { __builtin_amdgcn_raw_buffer_store_b64(d, r, v, s, AUX); }
template <int AUX> DEV void st32(u32 d, rsrc_t r, u32 v, u32 s) { __builtin_amdgcn_raw_buffer_store_b32(d, r, v, s, AUX); }
DEV void dma(u32 lds, u32 voff, rsrc_t r, u32 soff) {
  asm volatile("s_mov_b32 m0, %0\n\ts_nop 0\n\tbuffer_load_dwordx4 %1, %2, %3 offen lds" ::"s"(lds), "v"(voff), "s"(r), "s"(soff) : "memory");
}
#define BAR() asm volatile("s_barrier" ::: "memory")
#define BAR_LGKM() asm volatile("s_waitcnt lgkmcnt(0)\n\ts_barrier" ::: "memory")
#define BAR_ALL() asm volatile("s_waitcnt vmcnt(0) lgkmcnt(0)\n\ts_barrier" ::: "memory")
#define VM_BAR(n) asm volatile("s_waitcnt vmcnt(" #n ")\n\ts_barrier" ::: "memory")
DEV void swap32(u32 &a, u32 &b) { auto r = __builtin_amdgcn_permlane32_swap(a, b, false, false); a = r[0]; b = r[1]; }
DEV void swap16(u32 &a, u32 &b) { auto r = __builtin_amdgcn_permlane16_swap(a, b, false, false); a = r[0]; b = r[1]; }
DEV float halfmax(float v) {
  v = fmaxf(v, asf(__builtin_amdgcn_update_dpp(0u, asu(v), 0xB1, 0xF, 0xF, true)));
  v = fmaxf(v, asf(__builtin_amdgcn_update_dpp(0u, asu(v), 0x4E, 0xF, 0xF, true)));
  v = fmaxf(v, asf(__builtin_amdgcn_update_dpp(0u, asu(v), 0x141, 0xF, 0xF, true)));
  v = fmaxf(v, asf(__builtin_amdgcn_update_dpp(0u, asu(v), 0x140, 0xF, 0xF, true)));
  u32 a = asu(v), b = asu(v);
  swap16(a, b);
  return fmaxf(asf(a), asf(b));
}

DEV void group_sync(unsigned char *ws, int idx, int lane) {
  GAS u32 *c = (GAS u32 *)ws + 64 + idx * 64;
  if (lane == 0) __hip_atomic_fetch_add(c, 1u, RLX_AGENT);
  for (u32 spins = 0; spins < (1u << 22); ++spins) {
    u32 v = __hip_atomic_load(c, RLX_AGENT);
    if (__builtin_amdgcn_readfirstlane(v) >= 16u) break;
  }
  __builtin_amdgcn_fence(__ATOMIC_ACQUIRE, "wavefront");
}

DEV u32 norm1(u32 d, float rs, float nm) {
  float lo = __builtin_fmaf(asf(d << 16), rs, nm), hi = __builtin_fmaf(asf(d & 0xffff0000u), rs, nm);
  PIN(lo);
  return pk(lo, hi);
}
DEV void ln_finish(float a, float &rs, float &nm) {
  u32 x = asu(a), y = asu(a);
  swap32(x, y);
  const float mean = asf(x) * (1.0f / DM);
  const float var = fmaxf(__builtin_fmaf(-mean, mean, asf(y) * (1.0f / DM)), 0.f);
  rs = __builtin_amdgcn_rsqf(var + 1e-5f);
  nm = -mean * rs;
}
DEV void ln_from_partials(rsrc_t ws, u32 st_off, int token, int h, float &rs, float &nm) {
  const u32x4 p0 = ld128<SC1>(ws, token * 64 + h * 32, st_off), p1 = ld128<SC1>(ws, token * 64 + h * 32 + 16, st_off);
  const float a = ((asf(p0[0]) + asf(p0[1])) + (asf(p0[2]) + asf(p0[3]))) + ((asf(p1[0]) + asf(p1[1])) + (asf(p1[2]) + asf(p1[3])));
  ln_finish(a, rs, nm);
}

constexpr int XWIN = 20;
template <int LAYER> DEV void g1_compute(const Params &p, rsrc_t ws, u32 L, int r, int c, int cw, int lane) {
  FRESH(); SPIN(cw);
  const int h = lane >> 5, l31 = lane & 31;
  const int tile = 4 * r + cw;
  constexpr int AUX = LAYER ? SC1 : 0;
  const rsrc_t xr = LAYER ? ws : mkrs(p.x, SEQ * DM * 2);
  const u32 xv = LAYER ? lane * 16 : l31 * (DM * 2) + h * 16;
  const u32 xs = LAYER ? OFF_XT + XT_BYTES + tile * 40960 : tile * 32 * DM * 2;
  constexpr u32 XSTEP = LAYER ? 1024 : 32;
  u32x4 x[40];
  float rs, nm;
  if constexpr (LAYER == 0) {
#pragma unroll
    for (int i = 0; i < 40; ++i) x[i] = ld128<AUX>(xr, xv + i * XSTEP, xs);
    SBAR();
    float s = 0.f, q = 0.f;
#pragma unroll
    for (int i = 0; i < 40; ++i)
#pragma unroll
      for (int j = 0; j < 4; ++j) { s = dot2(x[i][j], 0x3F803F80u, s); q = dot2(x[i][j], x[i][j], q); }
    u32 a = asu(s), b = asu(q);
    swap32(a, b);
    ln_finish(asf(a) + asf(b), rs, nm);
    BAR();
  } else {
    ln_from_partials(ws, OFF_ST + ST_BYTES, tile * 32 + l31, h, rs, nm);
#pragma unroll
    for (int i = 0; i < XWIN; ++i) x[i] = ld128<AUX>(xr, xv + (i & 3) * XSTEP, xs + (i >> 2) * 4 * XSTEP);
    SBAR();
  }
  u32 wa[4];
  {
    const int G = (lane >> 4) & 1, q = (lane & 15) >> 2, pp = lane & 3, idx = 4 * G + pp;
    const u64 T[4] = {0x0706050403020100ull, 0x160B0A0F1409080Dull, 0x19181918170E150Cull, 0x1F1E1D1C13121110ull};
#pragma unroll
    for (int t = 0; t < 4; ++t) {
      const u32 pos = (u32)(T[t] >> (8 * idx)) & 31u;
      wa[t] = L + ((LAYER ? 4 : 8) * h + q) * 256 + ((pos << 3) ^ (u32)(q << 6));
    }
  }
  constexpr u32 I1 = LAYER ? 2048 : 1024;
  f32x16 acc[4];
#pragma unroll
  for (int t = 0; t < 4; ++t) acc[t] = f32x16{};
  u32x4 wf[4];
#pragma unroll
  for (int t = 0; t < 4; ++t) { const u32x2 a = ldtr(wa[t]), b = ldtr(wa[t] + I1); wf[t] = u32x4{a[0], a[1], b[0], b[1]}; }
  u32x4 xn;
#pragma unroll
  for (int j = 0; j < 4; ++j) xn[j] = norm1(x[0][j], rs, nm);
  SBAR();
#pragma unroll
  for (int s = 0; s < 40; ++s) {
    if ((s & 3) == 3 && s < 39) {
      BAR();
      if (s == 31) {
#pragma unroll
        for (int t = 0; t < 4; ++t) wa[t] -= 7 * 16384;
      } else {
#pragma unroll
        for (int t = 0; t < 4; ++t) wa[t] += 16384;
      }
#pragma unroll
      for (int t = 0; t < 4; ++t) PIN(wa[t]);
    }
    u32x4 xn2;
#pragma unroll
    for (int t = 0; t < 4; ++t) {
      acc[t] = mfma32(wf[t], xn, acc[t]);
      SBAR();
      if (s < 39) {
        const u32 o = ((s + 1) & 3) * 4096;
        const u32x2 a = ldtr(wa[t] + o), b = ldtr(wa[t] + o + I1);
        wf[t] = u32x4{a[0], a[1], b[0], b[1]};
        if (t == 0 && s + XWIN < 40) x[s + XWIN] = ld128<AUX>(xr, xv + ((s + XWIN) & 3) * XSTEP, xs + ((s + XWIN) >> 2) * 4 * XSTEP);
        SBAR();
        xn2[t] = norm1(x[s + 1][t], rs, nm);
        PIN(xn2[t]);
        SBAR();
      }
    }
    xn = xn2;
  }
  BAR();
  const u32 kv = OFF_KV + LAYER * KV_BYTES + (2 * c * 64 + tile) * REC;
  constexpr float QS = 0.22360679774997896f * 1.4426950408889634f;
  LAS char *qs = (LAS char *)(uintptr_t)(L + L_SCR + cw * 2560);
  u32x4 km[2];
#pragma unroll
  for (int hd = 0; hd < 2; ++hd) {
    km[hd] = u32x4{pk(acc[hd][8], acc[hd][9]), pk(acc[hd][10], acc[hd][11]), pk(acc[hd][12], acc[hd][13]), pk(acc[hd][14], acc[hd][15])};
    st128<SC1>(km[hd], ws, lane * 16, kv + hd * 64 * REC);
  }
  const u32x2 kt = u32x2{pk(acc[2][4], acc[2][5]), pk(acc[2][6], acc[2][7])};
  st64<SC1>(kt, ws, h * 64 * REC + 2368 + l31 * 8, kv);
  LAS char *vs = (LAS char *)(uintptr_t)(L + L_GAP + cw * 2560);
  float vm = 0.f;
#pragma unroll
  for (int g = 0; g < 4; ++g) {
    *(LAS u32x2 *)(vs + l31 * 80 + (2 * g + h) * 8) = u32x2{pk(acc[3][4 * g], acc[3][4 * g + 1]), pk(acc[3][4 * g + 2], acc[3][4 * g + 3])};
    vm = fmaxf(fmaxf(vm, fabsf(acc[3][4 * g])), fabsf(acc[3][4 * g + 1]));
    vm = fmaxf(fmaxf(vm, fabsf(acc[3][4 * g + 2])), fabsf(acc[3][4 * g + 3]));
  }
  *(LAS u32x2 *)(vs + l31 * 80 + (8 + h) * 8) = u32x2{pk(acc[2][8], acc[2][9]), pk(acc[2][10], acc[2][11])};
  vm = fmaxf(fmaxf(vm, fabsf(acc[2][8])), fabsf(acc[2][9]));
  vm = fmaxf(fmaxf(vm, fabsf(acc[2][10])), fabsf(acc[2][11]));
#pragma unroll
  for (int hd = 0; hd < 2; ++hd)
    *(LAS u32x4 *)(qs + hd * 1280 + lane * 16) = u32x4{pk(acc[hd][0] * QS, acc[hd][1] * QS), pk(acc[hd][2] * QS, acc[hd][3] * QS), pk(acc[hd][4] * QS, acc[hd][5] * QS), pk(acc[hd][6] * QS, acc[hd][7] * QS)};
  *(LAS u32x2 *)(qs + h * 1280 + 1024 + l31 * 8) = u32x2{pk(acc[2][0] * QS, acc[2][1] * QS), pk(acc[2][2] * QS, acc[2][3] * QS)};
  {
    const int G = (lane >> 4) & 1, q = (lane & 15) >> 2, pp = lane & 3;
    const u32 va = L + L_GAP + cw * 2560 + (4 * h + q) * 80 + G * 32 + pp * 8;
#pragma unroll
    for (int hd = 0; hd < 2; ++hd)
#pragma unroll
      for (int s = 0; s < 2; ++s) {
        const u32x2 a = ldtr(va + s * 1280 + hd * 40), b = ldtr(va + s * 1280 + 640 + hd * 40);
        u32x4 v = u32x4{a[0], a[1], b[0], b[1]};
        if (l31 >= 20) v = u32x4{0x3F803F80u, 0x3F803F80u, 0x3F803F80u, 0x3F803F80u};
        if (l31 <= 20) st128<SC1>(v, ws, 1024 + s * 672 + h * 336 + l31 * 16, kv + hd * 64 * REC);
      }
  }
  {
    float a = 0.f, b = 0.f;
#pragma unroll
    for (int j = 0; j < 4; ++j) { a = dot2(km[0][j], km[0][j], a); b = dot2(km[1][j], km[1][j], b); }
    const float t = dot2(kt[0], kt[0], dot2(kt[1], kt[1], 0.f));
    if (h) b += t; else a += t;
    if (!(vm <= 1.0e15f)) { a = __builtin_inff(); b = a; }
    u32 ua = asu(a), ub = asu(b);
    swap32(ua, ub);
    float m = asf(ua) + asf(ub);
    if (!(m == m)) m = __builtin_inff();
    m = halfmax(m);
    if (l31 == 0) st32<SC1>(asu(m), ws, h * 256, OFF_KM + LAYER * KM_BYTES + (2 * c * 64 + tile) * 4);
  }
}

#define EXP2(x) __builtin_amdgcn_exp2f(x)
DEV void attention(rsrc_t ws, u32 L, int layer, int r, int c, int w, int lane) {
  FRESH(); SPIN(w);
  const int h = lane >> 5, l31 = lane & 31;
  const int hb = w & 1, qp = (w >> 1) & 1, kh = w >> 2, head = 2 * c + hb;
  const u32 kvb = OFF_KV + layer * KV_BYTES + (head * 64 + 32 * kh) * REC;
  const u32 vK = lane * 16, vT = 2368 + l31 * 8, vV = 1024 + h * 336 + (l31 < 20 ? l31 : 20) * 16;
  u32x4 KA, KB, TA, TB, VA0, VA1, VB0, VB1;
  TA[2] = TA[3] = TB[2] = TB[3] = 0u;
#define LDK(K, T, t) do { K = ld128<SC1>(ws, vK, kvb + (t) * REC); const u32x2 t_ = ld64<SC1>(ws, vT, kvb + (t) * REC); T[0] = t_[0]; T[1] = t_[1]; } while (0)
#define LDV(V0, V1, t) do { V0 = ld128<SC1>(ws, vV, kvb + (t) * REC); V1 = ld128<SC1>(ws, vV + 672, kvb + (t) * REC); } while (0)
  const float kmx = asf(__builtin_amdgcn_raw_buffer_load_b32(ws, lane * 4, OFF_KM + layer * KM_BYTES + head * 256, SC1));
  LDK(KA, TA, 0); LDV(VA0, VA1, 0); LDK(KB, TB, 1); VB0 = ld128<SC1>(ws, vV, kvb + REC);
  VB1 = u32x4{0u, 0u, 0u, 0u};
  u32x4 qm[2], qt[2];
  float qn = 0.f;
#pragma unroll
  for (int i = 0; i < 2; ++i) {
    LAS char *qs = (LAS char *)(uintptr_t)(L + L_SCR + (2 * qp + i) * 2560 + hb * 1280);
    qm[i] = *(LAS u32x4 *)(qs + lane * 16);
    const u32x2 t = *(LAS u32x2 *)(qs + 1024 + l31 * 8);
    qt[i] = u32x4{h ? 0u : t[0], h ? 0u : t[1], 0u, 0u};
    float n = dot2(qt[i][0], qt[i][0], dot2(qt[i][1], qt[i][1], 0.f));
#pragma unroll
    for (int j = 0; j < 4; ++j) n = dot2(qm[i][j], qm[i][j], n);
    u32 a = asu(n), b = asu(n);
    swap32(a, b);
    qn = fmaxf(qn, asf(a) + asf(b));
    if (!(n == n)) qn = __builtin_inff();
  }
  float km2 = halfmax(kmx);
  { u32 a = asu(km2), b = asu(km2); swap32(a, b); km2 = fmaxf(asf(a), asf(b)); }
  const bool fast = __all(qn * km2 <= 4096.0f);
  f32x16 o0 = f32x16{}, o1 = f32x16{};
  float m0 = 0.f, m1 = 0.f;
  const f32x16 Z = f32x16{};
  if (__builtin_expect(fast, 1)) {
    f32x16 sa, sb = f32x16{};
    u32x4 p0, p1 = u32x4{0u, 0u, 0u, 0u};
    sa = mfma32(KA, qm[0], Z);
    sa = mfma32(TA, qt[0], sa);
#define UNIT(S, N, KM, KT, QN, OPREV, VPREV1, OCUR, VCUR0, L0, L1, L2, L3) do { \
    N = mfma32(KM, qm[QN], Z); SBAR(); L0; SBAR(); \
    S[0] = EXP2(S[0]); S[1] = EXP2(S[1]); S[2] = EXP2(S[2]); p0[0] = pk(S[0], S[1]); S[3] = EXP2(S[3]); PIN(S); PIN(p0); SBAR(); \
    OPREV = mfma32(VPREV1, p1, OPREV); SBAR(); L1; SBAR(); \
    S[4] = EXP2(S[4]); p0[1] = pk(S[2], S[3]); S[5] = EXP2(S[5]); S[6] = EXP2(S[6]); p0[2] = pk(S[4], S[5]); S[7] = EXP2(S[7]); PIN(S); PIN(p0); SBAR(); \
    N = mfma32(KT, qt[QN], N); SBAR(); L2; SBAR(); \
    S[8] = EXP2(S[8]); p0[3] = pk(S[6], S[7]); S[9] = EXP2(S[9]); S[10] = EXP2(S[10]); p1[0] = pk(S[8], S[9]); S[11] = EXP2(S[11]); PIN(S); PIN(p0); PIN(p1); SBAR(); \
    OCUR = mfma32(VCUR0, p0, OCUR); SBAR(); L3; SBAR(); \
    S[12] = EXP2(S[12]); p1[1] = pk(S[10], S[11]); S[13] = EXP2(S[13]); S[14] = EXP2(S[14]); p1[2] = pk(S[12], S[13]); S[15] = EXP2(S[15]); p1[3] = pk(S[14], S[15]); PIN(S); PIN(p1); SBAR(); \
  } while (0)
#define NOP do {} while (0)
#pragma unroll 1
    for (int t = 0; t < 32; t += 2) {
      const u32 s1 = kvb + (t + 1) * REC, s2 = s1 + REC, s3 = s2 + REC;
      UNIT(sa, sb, KA, TA, 1, o1, VB1, o0, VA0, NOP, VB1 = ld128<SC1>(ws, vV + 672, s1), NOP, KA = ld128<SC1>(ws, vK, s2));
      UNIT(sb, sa, KB, TB, 0, o0, VA1, o1, VA0, { const u32x2 t_ = ld64<SC1>(ws, vT, s2); TA[0] = t_[0]; TA[1] = t_[1]; }, NOP, NOP, VA0 = ld128<SC1>(ws, vV, s2));
      UNIT(sa, sb, KB, TB, 1, o1, VA1, o0, VB0, NOP, VA1 = ld128<SC1>(ws, vV + 672, s2), NOP, KB = ld128<SC1>(ws, vK, s3));
      UNIT(sb, sa, KA, TA, 0, o0, VB1, o1, VB0, { const u32x2 t_ = ld64<SC1>(ws, vT, s3); TB[0] = t_[0]; TB[1] = t_[1]; }, NOP, NOP, VB0 = ld128<SC1>(ws, vV, s3));
    }
    o1 = mfma32(VB1, p1, o1);
  } else {
    m0 = m1 = -3.0e38f;
#pragma unroll 1
    for (int t = 0; t < 32; ++t) {
      LDK(KA, TA, t); LDV(VA0, VA1, t);
#pragma unroll
      for (int i = 0; i < 2; ++i) {
        f32x16 s = mfma32(KA, qm[i], Z);
        s = mfma32(TA, qt[i], s);
        float mx = s[0];
#pragma unroll
        for (int j = 1; j < 16; ++j) mx = fmaxf(mx, s[j]);
        u32 a = asu(mx), b = asu(mx);
        swap32(a, b);
        float &m = i ? m1 : m0;
        f32x16 &o = i ? o1 : o0;
        const float mn = fmaxf(m, fmaxf(asf(a), asf(b)));
        const float al = EXP2(m - mn);
        m = mn;
#pragma unroll
        for (int j = 0; j < 16; ++j) { s[j] = EXP2(s[j] - mn); o[j] *= al; }
        o = mfma32(VA0, u32x4{pk(s[0], s[1]), pk(s[2], s[3]), pk(s[4], s[5]), pk(s[6], s[7])}, o);
        o = mfma32(VA1, u32x4{pk(s[8], s[9]), pk(s[10], s[11]), pk(s[12], s[13]), pk(s[14], s[15])}, o);
      }
    }
  }
  f32x16 mine = kh ? o1 : o0, other = kh ? o0 : o1;
  const float mmine = kh ? m1 : m0;
  if (h) other[9] = kh ? m0 : m1;
  LAS char *cs = (LAS char *)(uintptr_t)(L + (w < 4 ? L_GAP + w * 3072 : L_SCR + (w - 4) * 3072));
  LAS char *cr = (LAS char *)(uintptr_t)(L + (w < 4 ? L_SCR + w * 3072 : L_GAP + (w - 4) * 3072));
  BAR_LGKM();
#pragma unroll
  for (int j = 0; j < 3; ++j) *(LAS f32x4 *)(cs + j * 1024 + lane * 16) = f32x4{other[4 * j], other[4 * j + 1], other[4 * j + 2], other[4 * j + 3]};
  BAR_LGKM();
  f32x4 in[3];
#pragma unroll
  for (int j = 0; j < 3; ++j) in[j] = *(LAS f32x4 *)(cr + j * 1024 + lane * 16);
  float fa = 1.f, fb = 1.f;
  if (!fast) {
    u32 a = asu(in[2][1]), b = a;
    swap32(a, b);
    const float mo = asf(b), mn = fmaxf(mo, mmine);
    fa = EXP2(mmine - mn);
    fb = EXP2(mo - mn);
  }
  float o[12];
#pragma unroll
  for (int j = 0; j < 12; ++j) o[j] = fast ? mine[j] + in[j >> 2][j & 3] : mine[j] * fa + in[j >> 2][j & 3] * fb;
  u32 la = asu(o[8]), lb = la;
  swap32(la, lb);
  const float inv = __builtin_amdgcn_rcpf(asf(lb));
  const int token = 128 * r + 32 * (2 * qp + kh) + l31;
  const u32 ob = OFF_OT + layer * OT_BYTES;
  const u32 tb = (token >> 4) * 20480 + (token & 15) * 16;
#pragma unroll
  for (int j = 0; j < 3; ++j) {
    const int cg = 5 * head + 2 * j + (j < 2 ? h : 0);
    const u32 off = tb + (cg >> 3) * 1024 + (((cg >> 2) & 1) * 2 + (cg & 1)) * 256 + ((cg >> 1) & 1) * 8;
    const u32x2 v = u32x2{pk(o[4 * j] * inv, o[4 * j + 1] * inv), pk(o[4 * j + 2] * inv, o[4 * j + 3] * inv)};
    if (j < 2 || h == 0) st64<SC1>(v, ws, off, ob);
  }
}

template <int NK, int SLOT0, bool BARS>
DEV void gd_compute(const Params &p, rsrc_t ws, u32 L, u32 act_off, int xbuf, bool last, int r, int c, int cw, int lane, u32 (&res)[10]) {
  FRESH(); SPIN(cw);
  const int th = cw >> 1, kh = cw & 1, g = lane >> 4, l15 = lane & 15;
  const int t0 = 128 * r + 64 * (c & 1) + 32 * th;
  constexpr int WIN = NK < 18 ? NK : 18;
  const u32 as0 = act_off + ((t0 >> 4) * 2 * NK + kh * NK) * 1024, as1 = as0 + 2 * NK * 1024;
  const u32 av = lane * 16;
  u32x4 a0[NK], a1[NK];
#pragma unroll
  for (int i = 0; i < WIN; ++i) {
    a0[i] = ld128<SC1>(ws, av + (i & 3) * 1024, as0 + (i >> 2) * 4096);
    a1[i] = ld128<SC1>(ws, av + (i & 3) * 1024, as1 + (i >> 2) * 4096);
  }
  SBAR();
  u32 wa = L + SLOT0 * 20480 + kh * 10240 + (16 * (g >> 1) + 4 * (g & 1) + (l15 >> 2)) * 160 + (lane & 3) * 8;
  f32x4 acc[2][5];
  u32x4 wf[5];
#pragma unroll
  for (int j = 0; j < 5; ++j) {
    acc[0][j] = f32x4{}; acc[1][j] = f32x4{};
    const u32x2 a = ldtr(wa + 32 * j), b = ldtr(wa + 32 * j + 1280);
    wf[j] = u32x4{a[0], a[1], b[0], b[1]};
  }
  SBAR();
#pragma unroll
  for (int s = 0; s < NK; ++s) {
    if ((s & 1) && s + 1 < NK) {
      if (BARS) BAR();
      if ((SLOT0 + (s + 1) / 2) % 7 == 0) wa -= 6 * 20480; else wa += 20480;
      PIN(wa);
    }
#pragma unroll
    for (int j = 0; j < 5; ++j) {
      acc[0][j] = mfma16(wf[j], a0[s], acc[0][j]);
      SBAR();
      if (s + WIN < NK) {
        if (j == 0) a0[s + WIN] = ld128<SC1>(ws, av + ((s + WIN) & 3) * 1024, as0 + ((s + WIN) >> 2) * 4096);
        if (j == 2) a1[s + WIN] = ld128<SC1>(ws, av + ((s + WIN) & 3) * 1024, as1 + ((s + WIN) >> 2) * 4096);
      }
      SBAR();
      acc[1][j] = mfma16(wf[j], a1[s], acc[1][j]);
      SBAR();
      if (s + 1 < NK) {
        const u32 o = ((s + 1) & 1) * 5120 + 32 * j;
        const u32x2 a = ldtr(wa + o), b = ldtr(wa + o + 1280);
        wf[j] = u32x4{a[0], a[1], b[0], b[1]};
      }
      SBAR();
    }
  }
  LAS char *xs = (LAS char *)(uintptr_t)(L + L_SCR + cw * 5120), *xr = (LAS char *)(uintptr_t)(L + L_SCR + (cw ^ 1) * 5120);
#pragma unroll
  for (int j = 0; j < 5; ++j) *(LAS f32x4 *)(xs + j * 1024 + lane * 16) = kh ? acc[0][j] : acc[1][j];
  BAR_LGKM();
  float s1 = 0.f, s2 = 0.f;
#pragma unroll
  for (int j = 0; j < 5; ++j) {
    const f32x4 v = (kh ? acc[1][j] : acc[0][j]) + *(LAS f32x4 *)(xr + j * 1024 + lane * 16);
    res[2 * j] = pk(v[0] + asf(res[2 * j] << 16), v[1] + asf(res[2 * j] & 0xffff0000u));
    res[2 * j + 1] = pk(v[2] + asf(res[2 * j + 1] << 16), v[3] + asf(res[2 * j + 1] & 0xffff0000u));
  }
  const int tok = t0 + 16 * kh + l15, ct = c >> 1;
  if (last) {
    const rsrc_t ro = mkrs(p.out, SEQ * DM * 2);
#pragma unroll
    for (int j = 0; j < 5; ++j) st64<0>(u32x2{res[2 * j], res[2 * j + 1]}, ro, tok * (DM * 2) + g * 8 + j * 32, ct * 160);
  } else {
    const u32 xb = OFF_XT + xbuf * XT_BYTES + ((t0 >> 5) * 40 + 5 * ct) * 1024;
    const u32 xo = (g & 1) * 512 + (tok & 31) * 16;
#pragma unroll
    for (int j = 0; j < 4; j += 2) {
      u32 a0_ = res[2 * j], a1_ = res[2 * j + 1], b0_ = res[2 * j + 2], b1_ = res[2 * j + 3];
      swap32(a0_, b0_); swap32(a1_, b1_);
      st128<SC1>(u32x4{a0_, a1_, b0_, b1_}, ws, xo + (j + (g >> 1)) * 1024, xb);
    }
    st64<SC1>(u32x2{res[8], res[9]}, ws, xo + 4096 + (g >> 1) * 8, xb);
#pragma unroll
    for (int j = 0; j < 10; ++j) { s1 = dot2(res[j], 0x3F803F80u, s1); s2 = dot2(res[j], res[j], s2); }
    u32 a = asu(s1), b = asu(s2);
    swap32(a, b);
    a = asu(asf(a) + asf(b)); b = a;
    swap16(a, b);
    if (!(lane & 16)) st32<SC1>(asu(asf(a) + asf(b)), ws, tok * 64 + (lane >> 5) * 32 + ct * 4, OFF_ST + xbuf * ST_BYTES);
  }
}

DEV void g3_compute(rsrc_t ws, u32 L, int layer, int r, int c, int cw, int lane) {
  FRESH(); SPIN(cw);
  const int h = lane >> 5, l31 = lane & 31;
  const int tile = 4 * r + cw;
  const u32 xv = lane * 16, xs = OFF_XT + 2 * layer * XT_BYTES + tile * 40960;
  u32x4 x[40];
  float rs, nm;
  ln_from_partials(ws, OFF_ST + 2 * layer * ST_BYTES, tile * 32 + l31, h, rs, nm);
#pragma unroll
  for (int i = 0; i < XWIN; ++i) x[i] = ld128<SC1>(ws, xv + (i & 3) * 1024, xs + (i >> 2) * 4096);
  SBAR();
  u32 wa;
  {
    const int G = (lane >> 4) & 1, q = (lane & 15) >> 2, pp = lane & 3;
    wa = L + 4 * 28672 + G * 7168 + (4 * (h ^ G) + q) * 224 + pp * 8;
  }
  f32x16 acc[7];
  u32x4 wf[7];
#pragma unroll
  for (int t = 0; t < 7; ++t) {
    acc[t] = f32x16{};
    const u32x2 a = ldtr(wa + 32 * t), b = ldtr(wa + 32 * t + 1792);
    wf[t] = u32x4{a[0], a[1], b[0], b[1]};
  }
  u32x4 xn;
#pragma unroll
  for (int j = 0; j < 4; ++j) xn[j] = norm1(x[0][j], rs, nm);
  SBAR();
#pragma unroll
  for (int s = 0; s < 40; ++s) {
    if ((s & 3) == 3 && s < 39) {
      BAR();
      if ((4 + (s + 1) / 4) % 5 == 0) wa -= 4 * 28672; else wa += 28672;
      PIN(wa);
    }
    u32x4 xn2;
    float lo[4], hi[4];
#pragma unroll
    for (int t = 0; t < 7; ++t) {
      acc[t] = mfma32(wf[t], xn, acc[t]);
      SBAR();
      if (s < 39) {
        const u32 o = (((s + 1) >> 1) & 1) * 14336 + ((s + 1) & 1) * 3584 + 32 * t;
        const u32x2 a = ldtr(wa + o), b = ldtr(wa + o + 1792);
        wf[t] = u32x4{a[0], a[1], b[0], b[1]};
        if (t == 6 && s + XWIN < 40) x[s + XWIN] = ld128<SC1>(ws, xv + ((s + XWIN) & 3) * 1024, xs + ((s + XWIN) >> 2) * 4096);
        SBAR();
        const u32x4 xr = x[s + 1];
        if (t == 0) { lo[0] = asf(xr[0] << 16); hi[0] = asf(xr[0] & 0xffff0000u); lo[1] = asf(xr[1] << 16); hi[1] = asf(xr[1] & 0xffff0000u); PIN(lo[0]); PIN(hi[0]); PIN(lo[1]); PIN(hi[1]); }
        if (t == 1) { lo[0] = __builtin_fmaf(lo[0], rs, nm); PIN(lo[0]); hi[0] = __builtin_fmaf(hi[0], rs, nm); PIN(hi[0]); lo[1] = __builtin_fmaf(lo[1], rs, nm); PIN(lo[1]); }
        if (t == 2) { hi[1] = __builtin_fmaf(hi[1], rs, nm); PIN(hi[1]); xn2[0] = pk(lo[0], hi[0]); PIN(xn2[0]); lo[2] = asf(xr[2] << 16); PIN(lo[2]); }
        if (t == 3) { xn2[1] = pk(lo[1], hi[1]); PIN(xn2[1]); hi[2] = asf(xr[2] & 0xffff0000u); PIN(hi[2]); lo[3] = asf(xr[3] << 16); PIN(lo[3]); }
        if (t == 4) { hi[3] = asf(xr[3] & 0xffff0000u); PIN(hi[3]); lo[2] = __builtin_fmaf(lo[2], rs, nm); PIN(lo[2]); hi[2] = __builtin_fmaf(hi[2], rs, nm); PIN(hi[2]); }
        if (t == 5) { lo[3] = __builtin_fmaf(lo[3], rs, nm); PIN(lo[3]); hi[3] = __builtin_fmaf(hi[3], rs, nm); PIN(hi[3]); xn2[2] = pk(lo[2], hi[2]); PIN(xn2[2]); }
        if (t == 6) { xn2[3] = pk(lo[3], hi[3]); PIN(xn2[3]); }
        SBAR();
      }
    }
    xn = xn2;
  }
  BAR();
  const int f0 = (27 * c) >> 2, nt = ((27 * (c + 1)) >> 2) - f0;
  const int tok = tile * 32 + l31;
  const u32 hb = OFF_HT + layer * HT_BYTES;
  const u32 hv = (tok >> 4) * (54 * 1024) + h * 256 + (tok & 15) * 16;
#pragma unroll
  for (int t = 0; t < 7; ++t) {
    float v[8];
#pragma unroll
    for (int j = 0; j < 8; ++j) v[j] = fmaxf(acc[t][j], 0.f) * acc[t][8 + j];
    const int ft = f0 + t;
    if (t < 6 || nt == 7) st128<SC1>(u32x4{pk(v[0], v[1]), pk(v[2], v[3]), pk(v[4], v[5]), pk(v[6], v[7])}, ws, hv, hb + ft * 512);
  }
}

DEV void fill_g1(const Params &p, u32 L, int c, int hw, int lane, int s0, int s1) {
  PIN(lane); SPIN(c); SPIN(L); SPIN(hw);
  const rsrc_t rw = mkrs(p.wqkv, DM * 3 * DM * 2);
  const int q = lane >> 4, ls = (lane & 15) ^ (q << 2);
  const u32 code = (u32)(0xDCBE94BA72836510ull >> (4 * ls)) & 15u;
  const u32 seg = code >= 10 ? 2 : (code >= 5 ? 1 : 0);
  const u32 v = q * 3840 + seg * 1280 + (code - 5 * seg) * 16 + c * 80;
  for (int s = s0; s < s1; ++s)
#pragma unroll
    for (int i = 0; i < 4; ++i) dma(L + (s & 7) * 16384 + hw * 4096 + i * 1024, v, rw, (64 * s + 16 * hw + 4 * i) * 3840);
}
DEV void fill_gd(const unsigned short *w, int nk, int slot0, u32 L, int c, int hw, int lane, int s0, int s1) {
  PIN(lane); SPIN(c); SPIN(L); SPIN(hw);
  const rsrc_t rw = mkrs(w, nk * 64 * DM * 2);
  u32 v[5];
#pragma unroll
  for (int i = 0; i < 5; ++i) { const u32 idx = 64 * i + lane, row = idx / 10; v[i] = row * 1280 + (idx - row * 10) * 16 + (c >> 1) * 160; }
  for (int s = s0; s < s1; ++s) {
    const int ks = 2 * s + (hw & 1);
    if (ks < nk)
#pragma unroll
      for (int i = 0; i < 5; ++i) dma(L + ((slot0 + s) % 7) * 20480 + hw * 5120 + i * 1024, v[i], rw, ((hw >> 1) * nk + ks) * (32 * 1280));
  }
}
DEV void fill_g3(const Params &p, u32 L, int c, int hw, int lane, int s0, int s1) {
  PIN(lane); SPIN(c); SPIN(L); SPIN(hw);
  const rsrc_t rw = mkrs((hw & 1) ? p.wu : p.wg, DM * FF * 2);
  const int f0 = (27 * c) >> 2;
  u32 v[7];
#pragma unroll
  for (int i = 0; i < 7; ++i) { const u32 idx = 64 * i + lane, row = idx / 14; v[i] = (row ^ ((hw & 1) * 4)) * (FF * 2) + (idx - row * 14) * 16 + f0 * 32; }
  for (int s = s0; s < s1; ++s)
#pragma unroll
    for (int i = 0; i < 7; ++i) dma(L + ((4 + s) % 5) * 28672 + hw * 7168 + i * 1024, v[i], rw, (64 * s + 32 * (hw >> 1)) * (FF * 2));
}

__global__ __launch_bounds__(512, 2) void fused_kernel(Params p) {
  __shared__ __attribute__((aligned(1024))) char smem[L_BYTES];
  const u32 L = (u32)(uintptr_t)(LAS char *)smem;
  const int lane = threadIdx.x & 63, w = __builtin_amdgcn_readfirstlane(threadIdx.x >> 6);
  const int xcd = blockIdx.x & 7, bi = blockIdx.x >> 3;
  const int r = 4 * (xcd >> 1) + (bi >> 3), c = 8 * (xcd & 1) + (bi & 7);
  const rsrc_t ws = mkrs(p.ws, WS_USED);
  if (w < 4) {
    u32 res[10];
    {
      const rsrc_t xr = mkrs(p.x, SEQ * DM * 2);
      const int tok = 128 * r + 64 * (c & 1) + 32 * (w >> 1) + 16 * (w & 1) + (lane & 15);
#pragma unroll
      for (int j = 0; j < 5; ++j) { const u32x2 v = ld64<0>(xr, tok * (DM * 2) + (lane >> 4) * 8 + j * 32, (c >> 1) * 160); res[2 * j] = v[0]; res[2 * j + 1] = v[1]; }
    }
#pragma unroll 1
    for (int layer = 0; layer < 2; ++layer) {
      if (layer == 0) g1_compute<0>(p, ws, L, r, c, w, lane); else g1_compute<1>(p, ws, L, r, c, w, lane);
      BAR_ALL();
      if (w == 0) group_sync(p.ws, (layer * 5 + 0) * 16 + c, lane);
      BAR();
      attention(ws, L, layer, r, c, w, lane);
      BAR_ALL();
      if (w == 0) group_sync(p.ws, (layer * 5 + 1) * 16 + r, lane);
      BAR();
      gd_compute<10, 0, false>(p, ws, L, OFF_OT + layer * OT_BYTES, 2 * layer, false, r, c, w, lane, res);
      BAR_ALL();
      if (w == 0) group_sync(p.ws, (layer * 5 + 2) * 16 + r, lane);
      BAR();
      g3_compute(ws, L, layer, r, c, w, lane);
      BAR_ALL();
      if (w == 0) group_sync(p.ws, (layer * 5 + 3) * 16 + r, lane);
      BAR();
      gd_compute<27, 6, true>(p, ws, L, OFF_HT + layer * HT_BYTES, 1, layer == 1, r, c, w, lane, res);
      if (layer == 1) break;
      BAR_ALL();
      if (w == 0) group_sync(p.ws, (layer * 5 + 4) * 16 + r, lane);
      BAR();
    }
  } else {
    const int hw = w - 4;
    fill_g1(p, L, c, hw, lane, 0, 8);
    VM_BAR(28);
#pragma unroll 1
    for (int layer = 0; layer < 2; ++layer) {
      VM_BAR(24);
      VM_BAR(20); fill_g1(p, L, c, hw, lane, 8, 9);
      VM_BAR(20); fill_g1(p, L, c, hw, lane, 9, 10);
      VM_BAR(20); VM_BAR(16); VM_BAR(12); VM_BAR(8); VM_BAR(4); VM_BAR(0);
      BAR();
      fill_gd(p.wo, 10, 0, L, c, hw, lane, 0, 5);
      fill_g3(p, L, c, hw, lane, 0, 1);
      BAR(); BAR();
      attention(ws, L, layer, r, c, w, lane);
      BAR_ALL();
      BAR();
      BAR();
      fill_g3(p, L, c, hw, lane, 1, 5);
      BAR(); BAR();
      VM_BAR(21);
#pragma unroll 1
      for (int j = 2; j <= 6; ++j) { VM_BAR(14); fill_g3(p, L, c, hw, lane, j + 3, j + 4); }
      VM_BAR(14); fill_gd(p.wd, 27, 6, L, c, hw, lane, 0, 1);
      VM_BAR(12); fill_gd(p.wd, 27, 6, L, c, hw, lane, 1, 2);
      VM_BAR(10); fill_gd(p.wd, 27, 6, L, c, hw, lane, 2, 3);
      BAR();
      fill_gd(p.wd, 27, 6, L, c, hw, lane, 3, 7);
      BAR();
      VM_BAR(30);
      VM_BAR(25);
#pragma unroll 1
      for (int j = 2; j <= 8; ++j) { VM_BAR(20); fill_gd(p.wd, 27, 6, L, c, hw, lane, j + 5, j + 6); }
      VM_BAR(15); VM_BAR(10); VM_BAR(5); VM_BAR(0); VM_BAR(0);
      BAR();
      if (layer == 1) break;
      fill_g1(p, L, c, hw, lane, 0, 8);
      BAR();
      VM_BAR(28);
    }
  }
}

extern "C" void kernel_launch(void *const *d_in, const int *in_sizes, int n_in, void *d_out, int out_size, void *d_ws, size_t ws_size, hipStream_t stream) {
  Params p;
  p.x = (const unsigned short *)d_in[0];
  p.wqkv = (const unsigned short *)d_in[1];
  p.wo = (const unsigned short *)d_in[2];
  p.wg = (const unsigned short *)d_in[4];
  p.wu = (const unsigned short *)d_in[5];
  p.wd = (const unsigned short *)d_in[6];
  p.out = (unsigned short *)d_out;
  p.ws = (unsigned char *)d_ws;
  (void)hipMemsetAsync(d_ws, 0, CTRL_BYTES, stream);
  fused_kernel<<<256, 512, 0, stream>>>(p);
}
```

```cpp
#include <hip/hip_runtime.h>
#include <hip/hip_bf16.h>

typedef unsigned u32;
typedef unsigned long long u64;
typedef u32 u32x2 __attribute__((ext_vector_type(2)));
typedef u32 u32x4 __attribute__((ext_vector_type(4)));
typedef float f32x2 __attribute__((ext_vector_type(2)));
typedef float f32x4 __attribute__((ext_vector_type(4)));
typedef float f32x16 __attribute__((ext_vector_type(16)));
typedef __bf16 bf2 __attribute__((ext_vector_type(2)));
typedef __bf16 bf8 __attribute__((ext_vector_type(8)));
typedef short s16x4 __attribute__((ext_vector_type(4)));
typedef __amdgpu_buffer_rsrc_t rsrc_t;
#define GAS __attribute__((address_space(1)))
#define LAS __attribute__((address_space(3)))
#define DEV __device__ __forceinline__
#define SBAR() __builtin_amdgcn_sched_barrier(0)
#define PIN(x) asm volatile("" : "+v"(x))
#define SPIN(x) asm volatile("" : "+s"(x))
#define FRESH() do { PIN(lane); SPIN(r); SPIN(c); SPIN(L); } while (0)
#define RLX_AGENT __ATOMIC_RELAXED, __HIP_MEMORY_SCOPE_AGENT

constexpr int SEQ = 2048, DM = 640, FF = 1728;
constexpr int SC1 = 16;
constexpr u32 A_X2 = 0, A_S2 = 163840, A_H = 172032, A_X1 = 614400, A_S1 = 778240, A_O = 786432, ARENA = 950272;
constexpr u32 REC = 2688;
constexpr u32 KV_BYTES = 32 * 64 * REC;
constexpr u32 KM_BYTES = 32 * 64 * 4;
constexpr u32 OFF_FR = 360448, OFF_FC = 393216, OFF_TAG = 425984, CTRL_BYTES = 430080;
constexpr u32 OFF_AR = CTRL_BYTES;
constexpr u32 OFF_KV = OFF_AR + 48 * ARENA;
constexpr u32 OFF_KM = OFF_KV + 2 * KV_BYTES;
constexpr u32 OFF_W1 = OFF_KM + 2 * KM_BYTES;
constexpr u32 OFF_W3 = OFF_W1 + 16 * 40 * 4096;
constexpr u32 OFF_C3 = OFF_W3 + 16 * 40 * 7168;
constexpr u32 OFF_AG = OFF_C3 + 16 * 40 * 1024;
constexpr u32 OFF_W2 = OFF_AG + 256 * 12288;
constexpr u32 OFF_W4 = OFF_W2 + 8 * 5 * 20480;
constexpr u32 WS_USED = OFF_W4 + 8 * 14 * 20480;
constexpr u32 OOB = 0x80000000u;
constexpr u32 L_KV = 81920, L_SCR = 143360, L_GAP = 102400, L_BYTES = 163840;

struct Params {
  const unsigned short *x, *wqkv, *wo, *wg, *wu, *wd;
  unsigned short *out;
  unsigned char *ws;
};

template <int I> struct IC { static constexpr int v = I; };
template <int I, int N, class F> DEV void sfor(F &&f) { if constexpr (I < N) { f(IC<I>{}); sfor<I + 1, N>(f); } }
DEV u32 umax(u32 a, u32 b) { return a > b ? a : b; }
DEV float asf(u32 u) { return __builtin_bit_cast(float, u); }
DEV u32 asu(float f) { return __builtin_bit_cast(u32, f); }
DEV u32 pk(float lo, float hi) { f32x2 v = {lo, hi}; return __builtin_bit_cast(u32, __builtin_convertvector(v, bf2)); }
DEV f32x16 mfma32(u32x4 a, u32x4 b, f32x16 c) { return __builtin_amdgcn_mfma_f32_32x32x16_bf16(__builtin_bit_cast(bf8, a), __builtin_bit_cast(bf8, b), c, 0, 0, 0); }
DEV f32x4 mfma16(u32x4 a, u32x4 b, f32x4 c) { return __builtin_amdgcn_mfma_f32_16x16x32_bf16(__builtin_bit_cast(bf8, a), __builtin_bit_cast(bf8, b), c, 0, 0, 0); }
DEV u32x2 ldtr(u32 addr) { return __builtin_bit_cast(u32x2, __builtin_amdgcn_ds_read_tr16_b64_v4i16((LAS s16x4 *)(uintptr_t)addr)); }
DEV u32x4 lds128(u32 addr) { return *(LAS u32x4 *)(uintptr_t)addr; }
DEV float dot2(u32 a, u32 b, float c) { return __builtin_amdgcn_fdot2_f32_bf16(__builtin_bit_cast(bf2, a), __builtin_bit_cast(bf2, b), c, false); }
DEV rsrc_t mkrs(const void *p, u32 bytes) { return __builtin_amdgcn_make_buffer_rsrc((void *)p, 0, bytes, 0x00020000); }
template <int AUX> DEV u32x4 ld128(rsrc_t r, u32 v, u32 s) { return __builtin_amdgcn_raw_buffer_load_b128(r, v, s, AUX); }
template <int AUX> DEV u32x2 ld64(rsrc_t r, u32 v, u32 s) { return __builtin_amdgcn_raw_buffer_load_b64(r, v, s, AUX); }
template <int AUX> DEV void st128(u32x4 d, rsrc_t r, u32 v, u32 s) {
  __builtin_amdgcn_raw_buffer_store_b128(d, r, v, s, AUX);
  asm volatile("s_nop 1" ::"v"(d) : "memory");
}
template <int AUX> DEV void st64(u32x2 d, rsrc_t r, u32 v, u32 s) { __builtin_amdgcn_raw_buffer_store_b64(d, r, v, s, AUX); }
template <int AUX> DEV void st32(u32 d, rsrc_t r, u32 v, u32 s) { __builtin_amdgcn_raw_buffer_store_b32(d, r, v, s, AUX); }
DEV void dma(u32 lds, u32 voff, rsrc_t r, u32 soff) {
  asm volatile("s_mov_b32 m0, %0\n\ts_nop 0\n\tbuffer_load_dwordx4 %1, %2, %3 offen sc1 lds" ::"s"(lds), "v"(voff), "s"(r), "s"(soff) : "memory");
}
template <int N, int SL> DEV void dman(u32 lds, u32 voff, rsrc_t r, u32 soff) {
#define DMA_P(o) "buffer_load_dwordx4 %1, %2, %3 offen offset:" #o " sc1 lds\n\ts_sleep %4\n\t"
#define DMA_Q(o) "buffer_load_dwordx4 %1, %2, %3 offen offset:" #o " sc1 lds\n\t"
#define DMA_GO(txt) asm volatile("s_mov_b32 m0, %0\n\ts_nop 0\n\t" txt ::"s"(lds), "v"(voff), "s"(r), "s"(soff), "i"(SL) : "memory")
  if constexpr (SL > 0) {
    if constexpr (N == 1) DMA_GO(DMA_P(0));
    if constexpr (N == 2) DMA_GO(DMA_P(0) DMA_P(1024));
    if constexpr (N == 3) DMA_GO(DMA_P(0) DMA_P(1024) DMA_P(2048));
    if constexpr (N == 4) DMA_GO(DMA_P(0) DMA_P(1024) DMA_P(2048) DMA_P(3072));
  } else {
    if constexpr (N == 1) DMA_GO(DMA_Q(0));
    if constexpr (N == 2) DMA_GO(DMA_Q(0) DMA_Q(1024));
    if constexpr (N == 3) DMA_GO(DMA_Q(0) DMA_Q(1024) DMA_Q(2048));
    if constexpr (N == 4) DMA_GO(DMA_Q(0) DMA_Q(1024) DMA_Q(2048) DMA_Q(3072));
  }
}
#define TOUCH(d, voff, r, soff) asm volatile("buffer_load_dword %0, %1, %2, %3 offen" : "+v"(d) : "v"(voff), "s"(r), "s"(soff) : "memory")
#define TOUCH_DONE(d) asm volatile("" ::"v"(d))
DEV rsrc_t code_rsrc() {
  u64 pc;
  int d, n;
  asm volatile("s_getpc_b64 %0\n.Lpc%=:\n\ts_mov_b32 %1, _Z12fused_kernel6Params-.Lpc%=\n\ts_mov_b32 %2, _Z11end_of_codev-_Z12fused_kernel6Params" : "=s"(pc), "=s"(d), "=s"(n));
  return mkrs((const void *)(pc + (long long)d), n < 0 ? 0 : n);
}
#define BAR() asm volatile("s_barrier" ::: "memory")
#define BAR_LGKM() asm volatile("s_waitcnt lgkmcnt(0)\n\ts_barrier" ::: "memory")
#define BAR_ALL() asm volatile("s_waitcnt vmcnt(0) lgkmcnt(0)\n\ts_barrier" ::: "memory")
#define VM_BAR(n) asm volatile("s_waitcnt vmcnt(" #n ")\n\ts_barrier" ::: "memory")
DEV void swap32(u32 &a, u32 &b) { auto r = __builtin_amdgcn_permlane32_swap(a, b, false, false); a = r[0]; b = r[1]; }
DEV void swap16(u32 &a, u32 &b) { auto r = __builtin_amdgcn_permlane16_swap(a, b, false, false); a = r[0]; b = r[1]; }
DEV u32 halfmax_u(u32 v) {
  v = umax(v, (u32)__builtin_amdgcn_update_dpp(0u, v, 0xB1, 0xF, 0xF, true));
  v = umax(v, (u32)__builtin_amdgcn_update_dpp(0u, v, 0x4E, 0xF, 0xF, true));
  v = umax(v, (u32)__builtin_amdgcn_update_dpp(0u, v, 0x141, 0xF, 0xF, true));
  v = umax(v, (u32)__builtin_amdgcn_update_dpp(0u, v, 0x140, 0xF, 0xF, true));
  u32 b = v;
  swap16(v, b);
  return umax(v, b);
}
DEV bool unset(u32x4 v) { return umax(umax(v[0], v[1]), umax(v[2], v[3])) == 0xFFFFFFFFu; }
DEV bool unset(u32x2 v) { return umax(v[0], v[1]) == 0xFFFFFFFFu; }
#define RCOUNT()
#define RETRY(x, LOAD) do { for (u32 n_ = 0; n_ < 32768u; ++n_) { asm volatile("" ::: "memory"); RCOUNT(); x = LOAD; if (!__any(unset(x))) break; } } while (0)
#define RECHECK(x, LOAD) do { if (__builtin_expect(__any(unset(x)), 0)) RETRY(x, LOAD); } while (0)
DEV bool unset2(u32x4 a, u32x4 b) { return umax(umax(umax(a[0], a[1]), umax(a[2], a[3])), umax(umax(b[0], b[1]), umax(b[2], b[3]))) == 0xFFFFFFFFu; }
#define CHECK1(m, x) do { m = __builtin_amdgcn_ballot_w64(unset(x)); SPIN(m); } while (0)
#define CHECK2(m, x, LOAD) do { if (__builtin_expect(m != 0, 0)) RETRY(x, LOAD); } while (0)
#define STAMP(i)
#define STAMPR(i)
#define SARG
#define SPASS
#define ATT_ASM \
  "s_setprio 3\n\t" \
  "s_nop 0\n\t" \
  "v_mfma_f32_32x32x4_2b_bf16 v[0:31], v[194:195], v[116:117], 0\n\t" \
  "s_nop 15\n\t" \
  "s_nop 2\n\t" \
  "v_mfma_f32_32x32x16_bf16 v[0:15], v[186:189], v[108:111], v[0:15]\n\t" \
  "v_mfma_f32_32x32x16_bf16 v[16:31], v[190:193], v[108:111], v[16:31]\n\t" \
  "s_nop 10\n\t" \
  "v_mov_b32 v132, 1\n\t" \
  "v_exp_f32 v0, v0\n\t" \
  "v_exp_f32 v1, v1\n\t" \
  "v_exp_f32 v2, v2\n\t" \
  "v_exp_f32 v3, v3\n\t" \
  "v_cvt_pk_bf16_f32 v96, v0, v1\n\t" \
  "v_mfma_f32_32x32x4_2b_bf16 v[32:63], v[194:195], v[118:119], 0\n\t" \
  "s_mov_b32 %[cnt], 4\n\t" \
  "v_exp_f32 v4, v4\n\t" \
  "v_exp_f32 v5, v5\n\t" \
  "v_cvt_pk_bf16_f32 v97, v2, v3\n\t" \
  "v_exp_f32 v6, v6\n\t" \
  "v_exp_f32 v7, v7\n\t" \
  "v_cvt_pk_bf16_f32 v98, v4, v5\n\t" \
  "s_mov_b32 %[fast], 0\n\t" \
  "v_exp_f32 v8, v8\n\t" \
  "v_exp_f32 v9, v9\n\t" \
  "v_cvt_pk_bf16_f32 v99, v6, v7\n\t" \
  "v_exp_f32 v10, v10\n\t" \
  "v_exp_f32 v11, v11\n\t" \
  "v_cvt_pk_bf16_f32 v100, v8, v9\n\t" \
  "v_exp_f32 v12, v12\n\t" \
  "v_exp_f32 v13, v13\n\t" \
  "v_cvt_pk_bf16_f32 v101, v10, v11\n\t" \
  "v_exp_f32 v14, v14\n\t" \
  "v_exp_f32 v15, v15\n\t" \
  "v_cvt_pk_bf16_f32 v102, v12, v13\n\t" \
  "v_mfma_f32_32x32x16_bf16 v[64:79], v[196:199], v[96:99], 0\n\t" \
  "s_cmp_lg_u32 %[late], 0\n\t" \
  "s_cbranch_scc1 3f\n\t" \
  "s_waitcnt vmcnt(0)\n\t" \
  "buffer_atomic_add v132, v131, %[ws], %[sig] offen sc1\n\t" \
  "3:\n\t" \
  "v_exp_f32 v16, v16\n\t" \
  "v_exp_f32 v17, v17\n\t" \
  "v_cvt_pk_bf16_f32 v103, v14, v15\n\t" \
  "v_exp_f32 v18, v18\n\t" \
  "v_exp_f32 v19, v19\n\t" \
  "v_cvt_pk_bf16_f32 v96, v16, v17\n\t" \
  "v_mfma_f32_32x32x16_bf16 v[32:47], v[186:189], v[112:115], v[32:47]\n\t" \
  "v_exp_f32 v20, v20\n\t" \
  "v_exp_f32 v21, v21\n\t" \
  "v_cvt_pk_bf16_f32 v97, v18, v19\n\t" \
  "v_exp_f32 v22, v22\n\t" \
  "v_exp_f32 v23, v23\n\t" \
  "v_cvt_pk_bf16_f32 v98, v20, v21\n\t" \
  "v_mfma_f32_32x32x16_bf16 v[48:63], v[190:193], v[112:115], v[48:63]\n\t" \
  "v_exp_f32 v24, v24\n\t" \
  "v_exp_f32 v25, v25\n\t" \
  "v_cvt_pk_bf16_f32 v99, v22, v23\n\t" \
  "v_exp_f32 v26, v26\n\t" \
  "v_exp_f32 v27, v27\n\t" \
  "v_cvt_pk_bf16_f32 v104, v24, v25\n\t" \
  "v_mfma_f32_32x32x16_bf16 v[64:79], v[200:203], v[100:103], v[64:79]\n\t" \
  "v_exp_f32 v28, v28\n\t" \
  "v_exp_f32 v29, v29\n\t" \
  "v_cvt_pk_bf16_f32 v105, v26, v27\n\t" \
  "v_exp_f32 v30, v30\n\t" \
  "v_exp_f32 v31, v31\n\t" \
  "v_cvt_pk_bf16_f32 v106, v28, v29\n\t" \
  "s_cmp_eq_u32 %[wat], 3\n\t" \
  "s_cbranch_scc1 2f\n\t" \
  "s_barrier\n\t" \
  "2:\n\t" \
  "s_mov_b32 %[tmp2], 0x200000\n\t" \
  "7:\n\t" \
  "s_load_dword %[tmp], %[cptr], 0x0 glc\n\t" \
  "s_sub_u32 %[tmp2], %[tmp2], 1\n\t" \
  "s_cbranch_scc1 8f\n\t" \
  "s_waitcnt lgkmcnt(0)\n\t" \
  "s_cmp_lt_u32 %[tmp], 64\n\t" \
  "s_cbranch_scc1 7b\n\t" \
  "8:\n\t" \
  "s_waitcnt lgkmcnt(0)\n\t" \
  "buffer_load_dword v128, v129, %[ws], %[kmo] offen sc1\n\t" \
  "buffer_load_dwordx2 v[142:143], v121, %[ws], %[sk] offen sc1\n\t" \
  "buffer_load_dwordx4 v[134:137], v120, %[ws], %[sk] offen sc1\n\t" \
  "buffer_load_dwordx4 v[138:141], v120, %[ws], %[sk] offen offset:2688 sc1\n\t" \
  "buffer_load_dwordx4 v[144:147], v122, %[ws], %[sk] offen sc1\n\t" \
  "buffer_load_dwordx4 v[148:151], v122, %[ws], %[sk] offen offset:672 sc1\n\t" \
  "buffer_load_dwordx4 v[152:155], v122, %[ws], %[sk] offen offset:2688 sc1\n\t" \
  "buffer_load_dwordx4 v[156:159], v122, %[ws], %[sk] offen offset:3360 sc1\n\t" \
  "s_mov_b32 %[sv], %[sk]\n\t" \
  "s_add_u32 %[sk], %[sk], 5376\n\t" \
  "s_cmp_eq_u32 %[sk], %[kend]\n\t" \
  "s_cselect_b32 %[sk], %[kbeg], %[sk]\n\t" \
  "buffer_load_dwordx2 v[168:169], v121, %[ws], %[sk] offen sc1\n\t" \
  "buffer_load_dwordx4 v[160:163], v120, %[ws], %[sk] offen sc1\n\t" \
  "buffer_load_dwordx4 v[164:167], v120, %[ws], %[sk] offen offset:2688 sc1\n\t" \
  "buffer_load_dwordx4 v[170:173], v122, %[ws], %[sk] offen sc1\n\t" \
  "buffer_load_dwordx4 v[174:177], v122, %[ws], %[sk] offen offset:672 sc1\n\t" \
  "buffer_load_dwordx4 v[178:181], v122, %[ws], %[sk] offen offset:2688 sc1\n\t" \
  "buffer_load_dwordx4 v[182:185], v122, %[ws], %[sk] offen offset:3360 sc1\n\t" \
  "s_mov_b32 %[sv], %[sk]\n\t" \
  "s_add_u32 %[sk], %[sk], 5376\n\t" \
  "s_cmp_eq_u32 %[sk], %[kend]\n\t" \
  "s_cselect_b32 %[sk], %[kbeg], %[sk]\n\t" \
  "buffer_load_dwordx2 v[194:195], v121, %[ws], %[sk] offen sc1\n\t" \
  "buffer_load_dwordx4 v[186:189], v120, %[ws], %[sk] offen sc1\n\t" \
  "buffer_load_dwordx4 v[190:193], v120, %[ws], %[sk] offen offset:2688 sc1\n\t" \
  "s_mov_b32 %[sv], %[sk]\n\t" \
  "s_add_u32 %[sk], %[sk], 5376\n\t" \
  "s_cmp_eq_u32 %[sk], %[kend]\n\t" \
  "s_cselect_b32 %[sk], %[kbeg], %[sk]\n\t" \
  "v_mfma_f32_32x32x16_bf16 v[64:79], v[204:207], v[96:99], v[64:79]\n\t" \
  "v_mov_b32 v124, -1\n\t" \
  "v_exp_f32 v32, v32\n\t" \
  "v_exp_f32 v33, v33\n\t" \
  "v_cvt_pk_bf16_f32 v107, v30, v31\n\t" \
  "v_exp_f32 v34, v34\n\t" \
  "v_exp_f32 v35, v35\n\t" \
  "v_cvt_pk_bf16_f32 v96, v32, v33\n\t" \
  "v_mov_b32 v125, -1\n\t" \
  "v_exp_f32 v36, v36\n\t" \
  "v_exp_f32 v37, v37\n\t" \
  "v_cvt_pk_bf16_f32 v97, v34, v35\n\t" \
  "v_exp_f32 v38, v38\n\t" \
  "v_exp_f32 v39, v39\n\t" \
  "v_cvt_pk_bf16_f32 v98, v36, v37\n\t" \
  "v_mov_b32 v126, -1\n\t" \
  "v_exp_f32 v40, v40\n\t" \
  "v_exp_f32 v41, v41\n\t" \
  "v_cvt_pk_bf16_f32 v99, v38, v39\n\t" \
  "v_exp_f32 v42, v42\n\t" \
  "v_exp_f32 v43, v43\n\t" \
  "v_cvt_pk_bf16_f32 v100, v40, v41\n\t" \
  "v_mfma_f32_32x32x16_bf16 v[64:79], v[208:211], v[104:107], v[64:79]\n\t" \
  "v_mov_b32 v127, -1\n\t" \
  "v_exp_f32 v44, v44\n\t" \
  "v_exp_f32 v45, v45\n\t" \
  "v_cvt_pk_bf16_f32 v101, v42, v43\n\t" \
  "v_exp_f32 v46, v46\n\t" \
  "v_exp_f32 v47, v47\n\t" \
  "v_cvt_pk_bf16_f32 v102, v44, v45\n\t" \
  "v_mfma_f32_32x32x16_bf16 v[80:95], v[196:199], v[96:99], 0\n\t" \
  "buffer_load_dwordx4 v[196:199], v122, %[ws], %[sv] offen sc1\n\t" \
  "s_waitcnt vmcnt(18)\n\t" \
  "v_max_f32_dpp v128, v128, v128 row_shr:1 row_mask:0xf bank_mask:0xf\n\t" \
  "s_nop 1\n\t" \
  "v_max_f32_dpp v128, v128, v128 row_shr:2 row_mask:0xf bank_mask:0xf\n\t" \
  "v_exp_f32 v48, v48\n\t" \
  "v_exp_f32 v49, v49\n\t" \
  "v_cvt_pk_bf16_f32 v103, v46, v47\n\t" \
  "v_exp_f32 v50, v50\n\t" \
  "v_exp_f32 v51, v51\n\t" \
  "v_cvt_pk_bf16_f32 v96, v48, v49\n\t" \
  "v_max_f32_dpp v128, v128, v128 row_shr:4 row_mask:0xf bank_mask:0xf\n\t" \
  "s_nop 1\n\t" \
  "v_max_f32_dpp v128, v128, v128 row_shr:8 row_mask:0xf bank_mask:0xf\n\t" \
  "v_exp_f32 v52, v52\n\t" \
  "v_exp_f32 v53, v53\n\t" \
  "v_cvt_pk_bf16_f32 v97, v50, v51\n\t" \
  "v_exp_f32 v54, v54\n\t" \
  "v_exp_f32 v55, v55\n\t" \
  "v_cvt_pk_bf16_f32 v98, v52, v53\n\t" \
  "v_max_f32_dpp v128, v128, v128 row_bcast:15 row_mask:0xf bank_mask:0xf\n\t" \
  "s_nop 1\n\t" \
  "v_max_f32_dpp v128, v128, v128 row_bcast:31 row_mask:0xf bank_mask:0xf\n\t" \
  "v_exp_f32 v56, v56\n\t" \
  "v_exp_f32 v57, v57\n\t" \
  "v_cvt_pk_bf16_f32 v99, v54, v55\n\t" \
  "v_exp_f32 v58, v58\n\t" \
  "v_exp_f32 v59, v59\n\t" \
  "v_cvt_pk_bf16_f32 v104, v56, v57\n\t" \
  "v_mfma_f32_32x32x16_bf16 v[80:95], v[200:203], v[100:103], v[80:95]\n\t" \
  "buffer_load_dwordx4 v[200:203], v122, %[ws], %[sv] offen offset:672 sc1\n\t" \
  "v_readlane_b32 %[tmp], v128, 63\n\t" \
  "v_exp_f32 v60, v60\n\t" \
  "v_exp_f32 v61, v61\n\t" \
  "v_cvt_pk_bf16_f32 v105, v58, v59\n\t" \
  "v_exp_f32 v62, v62\n\t" \
  "v_exp_f32 v63, v63\n\t" \
  "v_cvt_pk_bf16_f32 v106, v60, v61\n\t" \
  "v_mul_f32 v128, %[tmp], v123\n\t" \
  "v_cmp_nge_f32 vcc, 0x45800000, v128\n\t" \
  "s_waitcnt vmcnt(16)\n\t" \
  "buffer_atomic_add v132, v133, %[ws], %[sig2] offen sc1\n\t" \
  "s_nop 2\n\t" \
  "s_cbranch_vccnz 9f\n\t" \
  "s_mov_b32 %[fast], 1\n\t" \
  "v_mfma_f32_32x32x4_2b_bf16 v[0:31], v[142:143], v[116:117], 0\n\t" \
  "s_nop 15\n\t" \
  "s_nop 2\n\t" \
  "v_mfma_f32_32x32x16_bf16 v[0:15], v[134:137], v[108:111], v[0:15]\n\t" \
  "v_mfma_f32_32x32x16_bf16 v[16:31], v[138:141], v[108:111], v[16:31]\n\t" \
  "s_nop 10\n\t" \
  ".p2align 6\n\t" \
  "1:\n\t" \
  "s_cmp_lg_u32 %[cnt], %[wat]\n\t" \
  "s_cbranch_scc1 6f\n\t" \
  "s_mov_b32 %[tmp2], 0x200000\n\t" \
  "5:\n\t" \
  "s_load_dword %[tmp], %[cptr], 0x50000 glc\n\t" \
  "s_waitcnt lgkmcnt(0)\n\t" \
  "s_sub_u32 %[tmp2], %[tmp2], 1\n\t" \
  "s_cbranch_scc1 6f\n\t" \
  "s_cmp_lt_u32 %[tmp], 128\n\t" \
  "s_cbranch_scc1 5b\n\t" \
  "6:\n\t" \
  "s_cmp_gt_u32 %[cnt], 2\n\t" \
  "s_cbranch_scc1 4f\n\t" \
  "s_setprio 2\n\t" \
  "s_cmp_eq_u32 %[cnt], 2\n\t" \
  "s_cbranch_scc1 4f\n\t" \
  "s_setprio 1\n\t" \
  "4:\n\t" \
  "v_mfma_f32_32x32x16_bf16 v[80:95], v[204:207], v[96:99], v[80:95]\n\t" \
  "buffer_load_dwordx4 v[204:207], v122, %[ws], %[sv] offen offset:2688 sc1\n\t" \
  "v_exp_f32 v0, v0\n\t" \
  "v_exp_f32 v1, v1\n\t" \
  "v_cvt_pk_bf16_f32 v107, v62, v63\n\t" \
  "v_exp_f32 v2, v2\n\t" \
  "v_exp_f32 v3, v3\n\t" \
  "v_cvt_pk_bf16_f32 v96, v0, v1\n\t" \
  "v_mfma_f32_32x32x4_2b_bf16 v[32:63], v[142:143], v[118:119], 0\n\t" \
  "buffer_load_dwordx2 v[142:143], v121, %[ws], %[sk] offen sc1\n\t" \
  "v_exp_f32 v4, v4\n\t" \
  "v_exp_f32 v5, v5\n\t" \
  "v_cvt_pk_bf16_f32 v97, v2, v3\n\t" \
  "v_exp_f32 v6, v6\n\t" \
  "v_exp_f32 v7, v7\n\t" \
  "v_cvt_pk_bf16_f32 v98, v4, v5\n\t" \
  "s_mov_b32 m0, %[ddst]\n\t" \
  "v_exp_f32 v8, v8\n\t" \
  "v_exp_f32 v9, v9\n\t" \
  "v_cvt_pk_bf16_f32 v99, v6, v7\n\t" \
  "v_exp_f32 v10, v10\n\t" \
  "v_exp_f32 v11, v11\n\t" \
  "v_cvt_pk_bf16_f32 v100, v8, v9\n\t" \
  "v_mfma_f32_32x32x16_bf16 v[80:95], v[208:211], v[104:107], v[80:95]\n\t" \
  "buffer_load_dwordx4 v[208:211], v122, %[ws], %[sv] offen offset:3360 sc1\n\t" \
  "v_exp_f32 v12, v12\n\t" \
  "v_exp_f32 v13, v13\n\t" \
  "v_cvt_pk_bf16_f32 v101, v10, v11\n\t" \
  "v_exp_f32 v14, v14\n\t" \
  "v_exp_f32 v15, v15\n\t" \
  "v_cvt_pk_bf16_f32 v102, v12, v13\n\t" \
  "s_waitcnt vmcnt(16)\n\t" \
  "v_mfma_f32_32x32x16_bf16 v[64:79], v[144:147], v[96:99], v[64:79]\n\t" \
  "buffer_load_dwordx4 v120, %[ws], %[dsrc] offen sc1 lds\n\t" \
  "v_exp_f32 v16, v16\n\t" \
  "v_exp_f32 v17, v17\n\t" \
  "v_cvt_pk_bf16_f32 v103, v14, v15\n\t" \
  "v_exp_f32 v18, v18\n\t" \
  "v_exp_f32 v19, v19\n\t" \
  "v_cvt_pk_bf16_f32 v96, v16, v17\n\t" \
  "v_mfma_f32_32x32x16_bf16 v[32:47], v[134:137], v[112:115], v[32:47]\n\t" \
  "buffer_load_dwordx4 v[134:137], v120, %[ws], %[sk] offen sc1\n\t" \
  "v_exp_f32 v20, v20\n\t" \
  "v_exp_f32 v21, v21\n\t" \
  "v_cvt_pk_bf16_f32 v97, v18, v19\n\t" \
  "v_exp_f32 v22, v22\n\t" \
  "v_exp_f32 v23, v23\n\t" \
  "v_cvt_pk_bf16_f32 v98, v20, v21\n\t" \
  "v_mfma_f32_32x32x16_bf16 v[48:63], v[138:141], v[112:115], v[48:63]\n\t" \
  "buffer_load_dwordx4 v[138:141], v120, %[ws], %[sk] offen offset:2688 sc1\n\t" \
  "v_exp_f32 v24, v24\n\t" \
  "v_exp_f32 v25, v25\n\t" \
  "v_cvt_pk_bf16_f32 v99, v22, v23\n\t" \
  "v_exp_f32 v26, v26\n\t" \
  "v_exp_f32 v27, v27\n\t" \
  "v_cvt_pk_bf16_f32 v104, v24, v25\n\t" \
  "v_mfma_f32_32x32x16_bf16 v[64:79], v[148:151], v[100:103], v[64:79]\n\t" \
  "s_mov_b32 %[sv], %[sk]\n\t" \
  "v_exp_f32 v28, v28\n\t" \
  "v_exp_f32 v29, v29\n\t" \
  "v_cvt_pk_bf16_f32 v105, v26, v27\n\t" \
  "v_exp_f32 v30, v30\n\t" \
  "v_exp_f32 v31, v31\n\t" \
  "v_cvt_pk_bf16_f32 v106, v28, v29\n\t" \
  "s_waitcnt vmcnt(16)\n\t" \
  "v_mfma_f32_32x32x16_bf16 v[64:79], v[152:155], v[96:99], v[64:79]\n\t" \
  "s_add_u32 %[sk], %[sk], 5376\n\t" \
  "v_exp_f32 v32, v32\n\t" \
  "v_exp_f32 v33, v33\n\t" \
  "v_cvt_pk_bf16_f32 v107, v30, v31\n\t" \
  "v_exp_f32 v34, v34\n\t" \
  "v_exp_f32 v35, v35\n\t" \
  "v_cvt_pk_bf16_f32 v96, v32, v33\n\t" \
  "v_mfma_f32_32x32x4_2b_bf16 v[0:31], v[168:169], v[116:117], 0\n\t" \
  "s_cmp_eq_u32 %[sk], %[kend]\n\t" \
  "s_cselect_b32 %[sk], %[kbeg], %[sk]\n\t" \
  "v_exp_f32 v36, v36\n\t" \
  "v_exp_f32 v37, v37\n\t" \
  "v_cvt_pk_bf16_f32 v97, v34, v35\n\t" \
  "v_exp_f32 v38, v38\n\t" \
  "v_exp_f32 v39, v39\n\t" \
  "v_cvt_pk_bf16_f32 v98, v36, v37\n\t" \
  "v_exp_f32 v40, v40\n\t" \
  "v_exp_f32 v41, v41\n\t" \
  "v_cvt_pk_bf16_f32 v99, v38, v39\n\t" \
  "v_exp_f32 v42, v42\n\t" \
  "v_exp_f32 v43, v43\n\t" \
  "v_cvt_pk_bf16_f32 v100, v40, v41\n\t" \
  "v_mfma_f32_32x32x16_bf16 v[64:79], v[156:159], v[104:107], v[64:79]\n\t" \
  "buffer_store_dwordx4 v[124:127], v120, %[ws], %[psrc] offen sc1\n\t" \
  "v_exp_f32 v44, v44\n\t" \
  "v_exp_f32 v45, v45\n\t" \
  "v_cvt_pk_bf16_f32 v101, v42, v43\n\t" \
  "v_exp_f32 v46, v46\n\t" \
  "v_exp_f32 v47, v47\n\t" \
  "v_cvt_pk_bf16_f32 v102, v44, v45\n\t" \
  "v_mfma_f32_32x32x16_bf16 v[80:95], v[144:147], v[96:99], v[80:95]\n\t" \
  "buffer_load_dwordx4 v[144:147], v122, %[ws], %[sv] offen sc1\n\t" \
  "v_exp_f32 v48, v48\n\t" \
  "v_exp_f32 v49, v49\n\t" \
  "v_cvt_pk_bf16_f32 v103, v46, v47\n\t" \
  "v_exp_f32 v50, v50\n\t" \
  "v_exp_f32 v51, v51\n\t" \
  "v_cvt_pk_bf16_f32 v96, v48, v49\n\t" \
  "v_mfma_f32_32x32x16_bf16 v[0:15], v[160:163], v[108:111], v[0:15]\n\t" \
  "buffer_load_dwordx4 v120, %[ws], %[dsrc] offen offset:1024 sc1 lds\n\t" \
  "v_exp_f32 v52, v52\n\t" \
  "v_exp_f32 v53, v53\n\t" \
  "v_cvt_pk_bf16_f32 v97, v50, v51\n\t" \
  "v_exp_f32 v54, v54\n\t" \
  "v_exp_f32 v55, v55\n\t" \
  "v_cvt_pk_bf16_f32 v98, v52, v53\n\t" \
  "v_mfma_f32_32x32x16_bf16 v[16:31], v[164:167], v[108:111], v[16:31]\n\t" \
  "v_exp_f32 v56, v56\n\t" \
  "v_exp_f32 v57, v57\n\t" \
  "v_cvt_pk_bf16_f32 v99, v54, v55\n\t" \
  "v_exp_f32 v58, v58\n\t" \
  "v_exp_f32 v59, v59\n\t" \
  "v_cvt_pk_bf16_f32 v104, v56, v57\n\t" \
  "v_mfma_f32_32x32x16_bf16 v[80:95], v[148:151], v[100:103], v[80:95]\n\t" \
  "buffer_load_dwordx4 v[148:151], v122, %[ws], %[sv] offen offset:672 sc1\n\t" \
  "v_exp_f32 v60, v60\n\t" \
  "v_exp_f32 v61, v61\n\t" \
  "v_cvt_pk_bf16_f32 v105, v58, v59\n\t" \
  "v_exp_f32 v62, v62\n\t" \
  "v_exp_f32 v63, v63\n\t" \
  "v_cvt_pk_bf16_f32 v106, v60, v61\n\t" \
  "v_mfma_f32_32x32x16_bf16 v[80:95], v[152:155], v[96:99], v[80:95]\n\t" \
  "buffer_load_dwordx4 v[152:155], v122, %[ws], %[sv] offen offset:2688 sc1\n\t" \
  "v_exp_f32 v0, v0\n\t" \
  "v_exp_f32 v1, v1\n\t" \
  "v_cvt_pk_bf16_f32 v107, v62, v63\n\t" \
  "v_exp_f32 v2, v2\n\t" \
  "v_exp_f32 v3, v3\n\t" \
  "v_cvt_pk_bf16_f32 v96, v0, v1\n\t" \
  "v_mfma_f32_32x32x4_2b_bf16 v[32:63], v[168:169], v[118:119], 0\n\t" \
  "buffer_load_dwordx2 v[168:169], v121, %[ws], %[sk] offen sc1\n\t" \
  "v_exp_f32 v4, v4\n\t" \
  "v_exp_f32 v5, v5\n\t" \
  "v_cvt_pk_bf16_f32 v97, v2, v3\n\t" \
  "v_exp_f32 v6, v6\n\t" \
  "v_exp_f32 v7, v7\n\t" \
  "v_cvt_pk_bf16_f32 v98, v4, v5\n\t" \
  "buffer_load_dword v128, v130, %[ws], %[pf] offen\n\t" \
  "v_exp_f32 v8, v8\n\t" \
  "v_exp_f32 v9, v9\n\t" \
  "v_cvt_pk_bf16_f32 v99, v6, v7\n\t" \
  "v_exp_f32 v10, v10\n\t" \
  "v_exp_f32 v11, v11\n\t" \
  "v_cvt_pk_bf16_f32 v100, v8, v9\n\t" \
  "v_mfma_f32_32x32x16_bf16 v[80:95], v[156:159], v[104:107], v[80:95]\n\t" \
  "buffer_load_dwordx4 v[156:159], v122, %[ws], %[sv] offen offset:3360 sc1\n\t" \
  "v_exp_f32 v12, v12\n\t" \
  "v_exp_f32 v13, v13\n\t" \
  "v_cvt_pk_bf16_f32 v101, v10, v11\n\t" \
  "v_exp_f32 v14, v14\n\t" \
  "v_exp_f32 v15, v15\n\t" \
  "v_cvt_pk_bf16_f32 v102, v12, v13\n\t" \
  "s_waitcnt vmcnt(19)\n\t" \
  "v_mfma_f32_32x32x16_bf16 v[64:79], v[170:173], v[96:99], v[64:79]\n\t" \
  "buffer_load_dwordx4 v120, %[ws], %[dsrc] offen offset:2048 sc1 lds\n\t" \
  "v_exp_f32 v16, v16\n\t" \
  "v_exp_f32 v17, v17\n\t" \
  "v_cvt_pk_bf16_f32 v103, v14, v15\n\t" \
  "v_exp_f32 v18, v18\n\t" \
  "v_exp_f32 v19, v19\n\t" \
  "v_cvt_pk_bf16_f32 v96, v16, v17\n\t" \
  "v_mfma_f32_32x32x16_bf16 v[32:47], v[160:163], v[112:115], v[32:47]\n\t" \
  "buffer_load_dwordx4 v[160:163], v120, %[ws], %[sk] offen sc1\n\t" \
  "v_exp_f32 v20, v20\n\t" \
  "v_exp_f32 v21, v21\n\t" \
  "v_cvt_pk_bf16_f32 v97, v18, v19\n\t" \
  "v_exp_f32 v22, v22\n\t" \
  "v_exp_f32 v23, v23\n\t" \
  "v_cvt_pk_bf16_f32 v98, v20, v21\n\t" \
  "v_mfma_f32_32x32x16_bf16 v[48:63], v[164:167], v[112:115], v[48:63]\n\t" \
  "buffer_load_dwordx4 v[164:167], v120, %[ws], %[sk] offen offset:2688 sc1\n\t" \
  "v_exp_f32 v24, v24\n\t" \
  "v_exp_f32 v25, v25\n\t" \
  "v_cvt_pk_bf16_f32 v99, v22, v23\n\t" \
  "v_exp_f32 v26, v26\n\t" \
  "v_exp_f32 v27, v27\n\t" \
  "v_cvt_pk_bf16_f32 v104, v24, v25\n\t" \
  "v_mfma_f32_32x32x16_bf16 v[64:79], v[174:177], v[100:103], v[64:79]\n\t" \
  "s_mov_b32 %[sv], %[sk]\n\t" \
  "v_exp_f32 v28, v28\n\t" \
  "v_exp_f32 v29, v29\n\t" \
  "v_cvt_pk_bf16_f32 v105, v26, v27\n\t" \
  "v_exp_f32 v30, v30\n\t" \
  "v_exp_f32 v31, v31\n\t" \
  "v_cvt_pk_bf16_f32 v106, v28, v29\n\t" \
  "s_waitcnt vmcnt(19)\n\t" \
  "v_mfma_f32_32x32x16_bf16 v[64:79], v[178:181], v[96:99], v[64:79]\n\t" \
  "s_add_u32 %[sk], %[sk], 5376\n\t" \
  "v_exp_f32 v32, v32\n\t" \
  "v_exp_f32 v33, v33\n\t" \
  "v_cvt_pk_bf16_f32 v107, v30, v31\n\t" \
  "v_exp_f32 v34, v34\n\t" \
  "v_exp_f32 v35, v35\n\t" \
  "v_cvt_pk_bf16_f32 v96, v32, v33\n\t" \
  "v_mfma_f32_32x32x4_2b_bf16 v[0:31], v[194:195], v[116:117], 0\n\t" \
  "s_cmp_eq_u32 %[sk], %[kend]\n\t" \
  "s_cselect_b32 %[sk], %[kbeg], %[sk]\n\t" \
  "v_exp_f32 v36, v36\n\t" \
  "v_exp_f32 v37, v37\n\t" \
  "v_cvt_pk_bf16_f32 v97, v34, v35\n\t" \
  "v_exp_f32 v38, v38\n\t" \
  "v_exp_f32 v39, v39\n\t" \
  "v_cvt_pk_bf16_f32 v98, v36, v37\n\t" \
  "s_cmp_eq_u32 %[cnt], 3\n\t" \
  "s_cselect_b32 %[tmp], %[pj], 2432\n\t" \
  "v_exp_f32 v40, v40\n\t" \
  "v_exp_f32 v41, v41\n\t" \
  "v_cvt_pk_bf16_f32 v99, v38, v39\n\t" \
  "v_exp_f32 v42, v42\n\t" \
  "v_exp_f32 v43, v43\n\t" \
  "v_cvt_pk_bf16_f32 v100, v40, v41\n\t" \
  "v_mfma_f32_32x32x16_bf16 v[64:79], v[182:185], v[104:107], v[64:79]\n\t" \
  "buffer_store_dwordx4 v[124:127], v120, %[ws], %[psrc] offen offset:1024 sc1\n\t" \
  "v_exp_f32 v44, v44\n\t" \
  "v_exp_f32 v45, v45\n\t" \
  "v_cvt_pk_bf16_f32 v101, v42, v43\n\t" \
  "v_exp_f32 v46, v46\n\t" \
  "v_exp_f32 v47, v47\n\t" \
  "v_cvt_pk_bf16_f32 v102, v44, v45\n\t" \
  "v_mfma_f32_32x32x16_bf16 v[80:95], v[170:173], v[96:99], v[80:95]\n\t" \
  "buffer_load_dwordx4 v[170:173], v122, %[ws], %[sv] offen sc1\n\t" \
  "v_exp_f32 v48, v48\n\t" \
  "v_exp_f32 v49, v49\n\t" \
  "v_cvt_pk_bf16_f32 v103, v46, v47\n\t" \
  "v_exp_f32 v50, v50\n\t" \
  "v_exp_f32 v51, v51\n\t" \
  "v_cvt_pk_bf16_f32 v96, v48, v49\n\t" \
  "v_mfma_f32_32x32x16_bf16 v[0:15], v[186:189], v[108:111], v[0:15]\n\t" \
  "s_add_u32 %[pf], %[pf], %[tmp]\n\t" \
  "v_exp_f32 v52, v52\n\t" \
  "v_exp_f32 v53, v53\n\t" \
  "v_cvt_pk_bf16_f32 v97, v50, v51\n\t" \
  "v_exp_f32 v54, v54\n\t" \
  "v_exp_f32 v55, v55\n\t" \
  "v_cvt_pk_bf16_f32 v98, v52, v53\n\t" \
  "v_mfma_f32_32x32x16_bf16 v[16:31], v[190:193], v[108:111], v[16:31]\n\t" \
  "s_add_u32 %[psrc], %[psrc], 2048\n\t" \
  "v_exp_f32 v56, v56\n\t" \
  "v_exp_f32 v57, v57\n\t" \
  "v_cvt_pk_bf16_f32 v99, v54, v55\n\t" \
  "v_exp_f32 v58, v58\n\t" \
  "v_exp_f32 v59, v59\n\t" \
  "v_cvt_pk_bf16_f32 v104, v56, v57\n\t" \
  "v_mfma_f32_32x32x16_bf16 v[80:95], v[174:177], v[100:103], v[80:95]\n\t" \
  "buffer_load_dwordx4 v[174:177], v122, %[ws], %[sv] offen offset:672 sc1\n\t" \
  "v_exp_f32 v60, v60\n\t" \
  "v_exp_f32 v61, v61\n\t" \
  "v_cvt_pk_bf16_f32 v105, v58, v59\n\t" \
  "v_exp_f32 v62, v62\n\t" \
  "v_exp_f32 v63, v63\n\t" \
  "v_cvt_pk_bf16_f32 v106, v60, v61\n\t" \
  "v_mfma_f32_32x32x16_bf16 v[80:95], v[178:181], v[96:99], v[80:95]\n\t" \
  "buffer_load_dwordx4 v[178:181], v122, %[ws], %[sv] offen offset:2688 sc1\n\t" \
  "v_exp_f32 v0, v0\n\t" \
  "v_exp_f32 v1, v1\n\t" \
  "v_cvt_pk_bf16_f32 v107, v62, v63\n\t" \
  "v_exp_f32 v2, v2\n\t" \
  "v_exp_f32 v3, v3\n\t" \
  "v_cvt_pk_bf16_f32 v96, v0, v1\n\t" \
  "v_mfma_f32_32x32x4_2b_bf16 v[32:63], v[194:195], v[118:119], 0\n\t" \
  "buffer_load_dwordx2 v[194:195], v121, %[ws], %[sk] offen sc1\n\t" \
  "v_exp_f32 v4, v4\n\t" \
  "v_exp_f32 v5, v5\n\t" \
  "v_cvt_pk_bf16_f32 v97, v2, v3\n\t" \
  "v_exp_f32 v6, v6\n\t" \
  "v_exp_f32 v7, v7\n\t" \
  "v_cvt_pk_bf16_f32 v98, v4, v5\n\t" \
  "s_cmp_eq_u32 %[cnt], 4\n\t" \
  "s_cselect_b32 %[tmp], %[dj0], 4096\n\t" \
  "s_cselect_b32 %[tmp2], %[dj3], 4096\n\t" \
  "v_exp_f32 v8, v8\n\t" \
  "v_exp_f32 v9, v9\n\t" \
  "v_cvt_pk_bf16_f32 v99, v6, v7\n\t" \
  "v_exp_f32 v10, v10\n\t" \
  "v_exp_f32 v11, v11\n\t" \
  "v_cvt_pk_bf16_f32 v100, v8, v9\n\t" \
  "v_mfma_f32_32x32x16_bf16 v[80:95], v[182:185], v[104:107], v[80:95]\n\t" \
  "buffer_load_dwordx4 v[182:185], v122, %[ws], %[sv] offen offset:3360 sc1\n\t" \
  "v_exp_f32 v12, v12\n\t" \
  "v_exp_f32 v13, v13\n\t" \
  "v_cvt_pk_bf16_f32 v101, v10, v11\n\t" \
  "v_exp_f32 v14, v14\n\t" \
  "v_exp_f32 v15, v15\n\t" \
  "v_cvt_pk_bf16_f32 v102, v12, v13\n\t" \
  "s_waitcnt vmcnt(20)\n\t" \
  "v_mfma_f32_32x32x16_bf16 v[64:79], v[196:199], v[96:99], v[64:79]\n\t" \
  "buffer_load_dwordx4 v120, %[ws], %[dsrc] offen offset:3072 sc1 lds\n\t" \
  "v_exp_f32 v16, v16\n\t" \
  "v_exp_f32 v17, v17\n\t" \
  "v_cvt_pk_bf16_f32 v103, v14, v15\n\t" \
  "v_exp_f32 v18, v18\n\t" \
  "v_exp_f32 v19, v19\n\t" \
  "v_cvt_pk_bf16_f32 v96, v16, v17\n\t" \
  "v_mfma_f32_32x32x16_bf16 v[32:47], v[186:189], v[112:115], v[32:47]\n\t" \
  "buffer_load_dwordx4 v[186:189], v120, %[ws], %[sk] offen sc1\n\t" \
  "v_exp_f32 v20, v20\n\t" \
  "v_exp_f32 v21, v21\n\t" \
  "v_cvt_pk_bf16_f32 v97, v18, v19\n\t" \
  "v_exp_f32 v22, v22\n\t" \
  "v_exp_f32 v23, v23\n\t" \
  "v_cvt_pk_bf16_f32 v98, v20, v21\n\t" \
  "v_mfma_f32_32x32x16_bf16 v[48:63], v[190:193], v[112:115], v[48:63]\n\t" \
  "buffer_load_dwordx4 v[190:193], v120, %[ws], %[sk] offen offset:2688 sc1\n\t" \
  "v_exp_f32 v24, v24\n\t" \
  "v_exp_f32 v25, v25\n\t" \
  "v_cvt_pk_bf16_f32 v99, v22, v23\n\t" \
  "v_exp_f32 v26, v26\n\t" \
  "v_exp_f32 v27, v27\n\t" \
  "v_cvt_pk_bf16_f32 v104, v24, v25\n\t" \
  "v_mfma_f32_32x32x16_bf16 v[64:79], v[200:203], v[100:103], v[64:79]\n\t" \
  "s_mov_b32 %[sv], %[sk]\n\t" \
  "v_exp_f32 v28, v28\n\t" \
  "v_exp_f32 v29, v29\n\t" \
  "v_cvt_pk_bf16_f32 v105, v26, v27\n\t" \
  "v_exp_f32 v30, v30\n\t" \
  "v_exp_f32 v31, v31\n\t" \
  "v_cvt_pk_bf16_f32 v106, v28, v29\n\t" \
  "s_waitcnt vmcnt(20)\n\t" \
  "v_mfma_f32_32x32x16_bf16 v[64:79], v[204:207], v[96:99], v[64:79]\n\t" \
  "s_add_u32 %[sk], %[sk], 5376\n\t" \
  "v_exp_f32 v32, v32\n\t" \
  "v_exp_f32 v33, v33\n\t" \
  "v_cvt_pk_bf16_f32 v107, v30, v31\n\t" \
  "v_exp_f32 v34, v34\n\t" \
  "v_exp_f32 v35, v35\n\t" \
  "v_cvt_pk_bf16_f32 v96, v32, v33\n\t" \
  "v_mfma_f32_32x32x4_2b_bf16 v[0:31], v[142:143], v[116:117], 0\n\t" \
  "s_cmp_eq_u32 %[sk], %[kend]\n\t" \
  "s_cselect_b32 %[sk], %[kbeg], %[sk]\n\t" \
  "v_exp_f32 v36, v36\n\t" \
  "v_exp_f32 v37, v37\n\t" \
  "v_cvt_pk_bf16_f32 v97, v34, v35\n\t" \
  "v_exp_f32 v38, v38\n\t" \
  "v_exp_f32 v39, v39\n\t" \
  "v_cvt_pk_bf16_f32 v98, v36, v37\n\t" \
  "s_cmp_eq_u32 %[cnt], 1\n\t" \
  "s_cselect_b32 %[tmp], %[dj1], %[tmp]\n\t" \
  "s_cselect_b32 %[tmp2], %[dj2], %[tmp2]\n\t" \
  "v_exp_f32 v40, v40\n\t" \
  "v_exp_f32 v41, v41\n\t" \
  "v_cvt_pk_bf16_f32 v99, v38, v39\n\t" \
  "v_exp_f32 v42, v42\n\t" \
  "v_exp_f32 v43, v43\n\t" \
  "v_cvt_pk_bf16_f32 v100, v40, v41\n\t" \
  "v_mfma_f32_32x32x16_bf16 v[64:79], v[208:211], v[104:107], v[64:79]\n\t" \
  "v_exp_f32 v44, v44\n\t" \
  "v_exp_f32 v45, v45\n\t" \
  "v_cvt_pk_bf16_f32 v101, v42, v43\n\t" \
  "v_exp_f32 v46, v46\n\t" \
  "v_exp_f32 v47, v47\n\t" \
  "v_cvt_pk_bf16_f32 v102, v44, v45\n\t" \
  "v_mfma_f32_32x32x16_bf16 v[80:95], v[196:199], v[96:99], v[80:95]\n\t" \
  "buffer_load_dwordx4 v[196:199], v122, %[ws], %[sv] offen sc1\n\t" \
  "v_exp_f32 v48, v48\n\t" \
  "v_exp_f32 v49, v49\n\t" \
  "v_cvt_pk_bf16_f32 v103, v46, v47\n\t" \
  "v_exp_f32 v50, v50\n\t" \
  "v_exp_f32 v51, v51\n\t" \
  "v_cvt_pk_bf16_f32 v96, v48, v49\n\t" \
  "v_mfma_f32_32x32x16_bf16 v[0:15], v[134:137], v[108:111], v[0:15]\n\t" \
  "s_add_u32 %[dsrc], %[dsrc], %[tmp]\n\t" \
  "v_exp_f32 v52, v52\n\t" \
  "v_exp_f32 v53, v53\n\t" \
  "v_cvt_pk_bf16_f32 v97, v50, v51\n\t" \
  "v_exp_f32 v54, v54\n\t" \
  "v_exp_f32 v55, v55\n\t" \
  "v_cvt_pk_bf16_f32 v98, v52, v53\n\t" \
  "v_mfma_f32_32x32x16_bf16 v[16:31], v[138:141], v[108:111], v[16:31]\n\t" \
  "s_add_u32 %[ddst], %[ddst], %[tmp2]\n\t" \
  "v_exp_f32 v56, v56\n\t" \
  "v_exp_f32 v57, v57\n\t" \
  "v_cvt_pk_bf16_f32 v99, v54, v55\n\t" \
  "v_exp_f32 v58, v58\n\t" \
  "v_exp_f32 v59, v59\n\t" \
  "v_cvt_pk_bf16_f32 v104, v56, v57\n\t" \
  "v_mfma_f32_32x32x16_bf16 v[80:95], v[200:203], v[100:103], v[80:95]\n\t" \
  "buffer_load_dwordx4 v[200:203], v122, %[ws], %[sv] offen offset:672 sc1\n\t" \
  "v_exp_f32 v60, v60\n\t" \
  "v_exp_f32 v61, v61\n\t" \
  "v_cvt_pk_bf16_f32 v105, v58, v59\n\t" \
  "v_exp_f32 v62, v62\n\t" \
  "v_exp_f32 v63, v63\n\t" \
  "v_cvt_pk_bf16_f32 v106, v60, v61\n\t" \
  "s_sub_u32 %[cnt], %[cnt], 1\n\t" \
  "s_cmp_lg_u32 %[cnt], 0\n\t" \
  "s_cbranch_scc1 1b\n\t" \
  "s_setprio 0\n\t" \
  "v_mfma_f32_32x32x16_bf16 v[80:95], v[204:207], v[96:99], v[80:95]\n\t" \
  "buffer_load_dwordx4 v[204:207], v122, %[ws], %[sv] offen offset:2688 sc1\n\t" \
  "v_exp_f32 v0, v0\n\t" \
  "v_exp_f32 v1, v1\n\t" \
  "v_cvt_pk_bf16_f32 v107, v62, v63\n\t" \
  "v_exp_f32 v2, v2\n\t" \
  "v_exp_f32 v3, v3\n\t" \
  "v_cvt_pk_bf16_f32 v96, v0, v1\n\t" \
  "v_mfma_f32_32x32x4_2b_bf16 v[32:63], v[142:143], v[118:119], 0\n\t" \
  "s_mov_b32 m0, %[ddst]\n\t" \
  "v_exp_f32 v4, v4\n\t" \
  "v_exp_f32 v5, v5\n\t" \
  "v_cvt_pk_bf16_f32 v97, v2, v3\n\t" \
  "v_exp_f32 v6, v6\n\t" \
  "v_exp_f32 v7, v7\n\t" \
  "v_cvt_pk_bf16_f32 v98, v4, v5\n\t" \
  "buffer_load_dwordx4 v120, %[ws], %[dsrc] offen sc1 lds\n\t" \
  "v_exp_f32 v8, v8\n\t" \
  "v_exp_f32 v9, v9\n\t" \
  "v_cvt_pk_bf16_f32 v99, v6, v7\n\t" \
  "v_exp_f32 v10, v10\n\t" \
  "v_exp_f32 v11, v11\n\t" \
  "v_cvt_pk_bf16_f32 v100, v8, v9\n\t" \
  "v_mfma_f32_32x32x16_bf16 v[80:95], v[208:211], v[104:107], v[80:95]\n\t" \
  "buffer_load_dwordx4 v[208:211], v122, %[ws], %[sv] offen offset:3360 sc1\n\t" \
  "v_exp_f32 v12, v12\n\t" \
  "v_exp_f32 v13, v13\n\t" \
  "v_cvt_pk_bf16_f32 v101, v10, v11\n\t" \
  "v_exp_f32 v14, v14\n\t" \
  "v_exp_f32 v15, v15\n\t" \
  "v_cvt_pk_bf16_f32 v102, v12, v13\n\t" \
  "s_waitcnt vmcnt(17)\n\t" \
  "v_mfma_f32_32x32x16_bf16 v[64:79], v[144:147], v[96:99], v[64:79]\n\t" \
  "buffer_load_dwordx4 v120, %[ws], %[dsrc] offen offset:1024 sc1 lds\n\t" \
  "v_exp_f32 v16, v16\n\t" \
  "v_exp_f32 v17, v17\n\t" \
  "v_cvt_pk_bf16_f32 v103, v14, v15\n\t" \
  "v_exp_f32 v18, v18\n\t" \
  "v_exp_f32 v19, v19\n\t" \
  "v_cvt_pk_bf16_f32 v96, v16, v17\n\t" \
  "v_mfma_f32_32x32x16_bf16 v[32:47], v[134:137], v[112:115], v[32:47]\n\t" \
  "buffer_load_dwordx4 v120, %[ws], %[dsrc] offen offset:2048 sc1 lds\n\t" \
  "v_exp_f32 v20, v20\n\t" \
  "v_exp_f32 v21, v21\n\t" \
  "v_cvt_pk_bf16_f32 v97, v18, v19\n\t" \
  "v_exp_f32 v22, v22\n\t" \
  "v_exp_f32 v23, v23\n\t" \
  "v_cvt_pk_bf16_f32 v98, v20, v21\n\t" \
  "v_mfma_f32_32x32x16_bf16 v[48:63], v[138:141], v[112:115], v[48:63]\n\t" \
  "buffer_load_dwordx4 v120, %[ws], %[dsrc] offen offset:3072 sc1 lds\n\t" \
  "v_exp_f32 v24, v24\n\t" \
  "v_exp_f32 v25, v25\n\t" \
  "v_cvt_pk_bf16_f32 v99, v22, v23\n\t" \
  "v_exp_f32 v26, v26\n\t" \
  "v_exp_f32 v27, v27\n\t" \
  "v_cvt_pk_bf16_f32 v104, v24, v25\n\t" \
  "v_mfma_f32_32x32x16_bf16 v[64:79], v[148:151], v[100:103], v[64:79]\n\t" \
  "buffer_load_dword v128, v130, %[ws], %[pf] offen\n\t" \
  "v_exp_f32 v28, v28\n\t" \
  "v_exp_f32 v29, v29\n\t" \
  "v_cvt_pk_bf16_f32 v105, v26, v27\n\t" \
  "v_exp_f32 v30, v30\n\t" \
  "v_exp_f32 v31, v31\n\t" \
  "v_cvt_pk_bf16_f32 v106, v28, v29\n\t" \
  "s_waitcnt vmcnt(18)\n\t" \
  "v_mfma_f32_32x32x16_bf16 v[64:79], v[152:155], v[96:99], v[64:79]\n\t" \
  "v_exp_f32 v32, v32\n\t" \
  "v_exp_f32 v33, v33\n\t" \
  "v_cvt_pk_bf16_f32 v107, v30, v31\n\t" \
  "v_exp_f32 v34, v34\n\t" \
  "v_exp_f32 v35, v35\n\t" \
  "v_cvt_pk_bf16_f32 v96, v32, v33\n\t" \
  "v_mfma_f32_32x32x4_2b_bf16 v[0:31], v[168:169], v[116:117], 0\n\t" \
  "v_exp_f32 v36, v36\n\t" \
  "v_exp_f32 v37, v37\n\t" \
  "v_cvt_pk_bf16_f32 v97, v34, v35\n\t" \
  "v_exp_f32 v38, v38\n\t" \
  "v_exp_f32 v39, v39\n\t" \
  "v_cvt_pk_bf16_f32 v98, v36, v37\n\t" \
  "v_exp_f32 v40, v40\n\t" \
  "v_exp_f32 v41, v41\n\t" \
  "v_cvt_pk_bf16_f32 v99, v38, v39\n\t" \
  "v_exp_f32 v42, v42\n\t" \
  "v_exp_f32 v43, v43\n\t" \
  "v_cvt_pk_bf16_f32 v100, v40, v41\n\t" \
  "v_mfma_f32_32x32x16_bf16 v[64:79], v[156:159], v[104:107], v[64:79]\n\t" \
  "v_exp_f32 v44, v44\n\t" \
  "v_exp_f32 v45, v45\n\t" \
  "v_cvt_pk_bf16_f32 v101, v42, v43\n\t" \
  "v_exp_f32 v46, v46\n\t" \
  "v_exp_f32 v47, v47\n\t" \
  "v_cvt_pk_bf16_f32 v102, v44, v45\n\t" \
  "v_mfma_f32_32x32x16_bf16 v[80:95], v[144:147], v[96:99], v[80:95]\n\t" \
  "v_exp_f32 v48, v48\n\t" \
  "v_exp_f32 v49, v49\n\t" \
  "v_cvt_pk_bf16_f32 v103, v46, v47\n\t" \
  "v_exp_f32 v50, v50\n\t" \
  "v_exp_f32 v51, v51\n\t" \
  "v_cvt_pk_bf16_f32 v96, v48, v49\n\t" \
  "v_mfma_f32_32x32x16_bf16 v[0:15], v[160:163], v[108:111], v[0:15]\n\t" \
  "v_exp_f32 v52, v52\n\t" \
  "v_exp_f32 v53, v53\n\t" \
  "v_cvt_pk_bf16_f32 v97, v50, v51\n\t" \
  "v_exp_f32 v54, v54\n\t" \
  "v_exp_f32 v55, v55\n\t" \
  "v_cvt_pk_bf16_f32 v98, v52, v53\n\t" \
  "v_mfma_f32_32x32x16_bf16 v[16:31], v[164:167], v[108:111], v[16:31]\n\t" \
  "v_exp_f32 v56, v56\n\t" \
  "v_exp_f32 v57, v57\n\t" \
  "v_cvt_pk_bf16_f32 v99, v54, v55\n\t" \
  "v_exp_f32 v58, v58\n\t" \
  "v_exp_f32 v59, v59\n\t" \
  "v_cvt_pk_bf16_f32 v104, v56, v57\n\t" \
  "v_mfma_f32_32x32x16_bf16 v[80:95], v[148:151], v[100:103], v[80:95]\n\t" \
  "v_exp_f32 v60, v60\n\t" \
  "v_exp_f32 v61, v61\n\t" \
  "v_cvt_pk_bf16_f32 v105, v58, v59\n\t" \
  "v_exp_f32 v62, v62\n\t" \
  "v_exp_f32 v63, v63\n\t" \
  "v_cvt_pk_bf16_f32 v106, v60, v61\n\t" \
  "v_mfma_f32_32x32x16_bf16 v[80:95], v[152:155], v[96:99], v[80:95]\n\t" \
  "v_exp_f32 v0, v0\n\t" \
  "v_exp_f32 v1, v1\n\t" \
  "v_cvt_pk_bf16_f32 v107, v62, v63\n\t" \
  "v_exp_f32 v2, v2\n\t" \
  "v_exp_f32 v3, v3\n\t" \
  "v_cvt_pk_bf16_f32 v96, v0, v1\n\t" \
  "v_mfma_f32_32x32x4_2b_bf16 v[32:63], v[168:169], v[118:119], 0\n\t" \
  "v_exp_f32 v4, v4\n\t" \
  "v_exp_f32 v5, v5\n\t" \
  "v_cvt_pk_bf16_f32 v97, v2, v3\n\t" \
  "v_exp_f32 v6, v6\n\t" \
  "v_exp_f32 v7, v7\n\t" \
  "v_cvt_pk_bf16_f32 v98, v4, v5\n\t" \
  "v_exp_f32 v8, v8\n\t" \
  "v_exp_f32 v9, v9\n\t" \
  "v_cvt_pk_bf16_f32 v99, v6, v7\n\t" \
  "v_exp_f32 v10, v10\n\t" \
  "v_exp_f32 v11, v11\n\t" \
  "v_cvt_pk_bf16_f32 v100, v8, v9\n\t" \
  "v_mfma_f32_32x32x16_bf16 v[80:95], v[156:159], v[104:107], v[80:95]\n\t" \
  "v_exp_f32 v12, v12\n\t" \
  "v_exp_f32 v13, v13\n\t" \
  "v_cvt_pk_bf16_f32 v101, v10, v11\n\t" \
  "v_exp_f32 v14, v14\n\t" \
  "v_exp_f32 v15, v15\n\t" \
  "v_cvt_pk_bf16_f32 v102, v12, v13\n\t" \
  "s_waitcnt vmcnt(12)\n\t" \
  "v_mfma_f32_32x32x16_bf16 v[64:79], v[170:173], v[96:99], v[64:79]\n\t" \
  "v_exp_f32 v16, v16\n\t" \
  "v_exp_f32 v17, v17\n\t" \
  "v_cvt_pk_bf16_f32 v103, v14, v15\n\t" \
  "v_exp_f32 v18, v18\n\t" \
  "v_exp_f32 v19, v19\n\t" \
  "v_cvt_pk_bf16_f32 v96, v16, v17\n\t" \
  "v_mfma_f32_32x32x16_bf16 v[32:47], v[160:163], v[112:115], v[32:47]\n\t" \
  "v_exp_f32 v20, v20\n\t" \
  "v_exp_f32 v21, v21\n\t" \
  "v_cvt_pk_bf16_f32 v97, v18, v19\n\t" \
  "v_exp_f32 v22, v22\n\t" \
  "v_exp_f32 v23, v23\n\t" \
  "v_cvt_pk_bf16_f32 v98, v20, v21\n\t" \
  "v_mfma_f32_32x32x16_bf16 v[48:63], v[164:167], v[112:115], v[48:63]\n\t" \
  "v_exp_f32 v24, v24\n\t" \
  "v_exp_f32 v25, v25\n\t" \
  "v_cvt_pk_bf16_f32 v99, v22, v23\n\t" \
  "v_exp_f32 v26, v26\n\t" \
  "v_exp_f32 v27, v27\n\t" \
  "v_cvt_pk_bf16_f32 v104, v24, v25\n\t" \
  "v_mfma_f32_32x32x16_bf16 v[64:79], v[174:177], v[100:103], v[64:79]\n\t" \
  "v_exp_f32 v28, v28\n\t" \
  "v_exp_f32 v29, v29\n\t" \
  "v_cvt_pk_bf16_f32 v105, v26, v27\n\t" \
  "v_exp_f32 v30, v30\n\t" \
  "v_exp_f32 v31, v31\n\t" \
  "v_cvt_pk_bf16_f32 v106, v28, v29\n\t" \
  "s_waitcnt vmcnt(9)\n\t" \
  "v_mfma_f32_32x32x16_bf16 v[64:79], v[178:181], v[96:99], v[64:79]\n\t" \
  "v_exp_f32 v32, v32\n\t" \
  "v_exp_f32 v33, v33\n\t" \
  "v_cvt_pk_bf16_f32 v107, v30, v31\n\t" \
  "v_exp_f32 v34, v34\n\t" \
  "v_exp_f32 v35, v35\n\t" \
  "v_cvt_pk_bf16_f32 v96, v32, v33\n\t" \
  "v_mfma_f32_32x32x4_2b_bf16 v[0:31], v[194:195], v[116:117], 0\n\t" \
  "v_exp_f32 v36, v36\n\t" \
  "v_exp_f32 v37, v37\n\t" \
  "v_cvt_pk_bf16_f32 v97, v34, v35\n\t" \
  "v_exp_f32 v38, v38\n\t" \
  "v_exp_f32 v39, v39\n\t" \
  "v_cvt_pk_bf16_f32 v98, v36, v37\n\t" \
  "v_exp_f32 v40, v40\n\t" \
  "v_exp_f32 v41, v41\n\t" \
  "v_cvt_pk_bf16_f32 v99, v38, v39\n\t" \
  "v_exp_f32 v42, v42\n\t" \
  "v_exp_f32 v43, v43\n\t" \
  "v_cvt_pk_bf16_f32 v100, v40, v41\n\t" \
  "v_mfma_f32_32x32x16_bf16 v[64:79], v[182:185], v[104:107], v[64:79]\n\t" \
  "v_exp_f32 v44, v44\n\t" \
  "v_exp_f32 v45, v45\n\t" \
  "v_cvt_pk_bf16_f32 v101, v42, v43\n\t" \
  "v_exp_f32 v46, v46\n\t" \
  "v_exp_f32 v47, v47\n\t" \
  "v_cvt_pk_bf16_f32 v102, v44, v45\n\t" \
  "v_mfma_f32_32x32x16_bf16 v[80:95], v[170:173], v[96:99], v[80:95]\n\t" \
  "v_exp_f32 v48, v48\n\t" \
  "v_exp_f32 v49, v49\n\t" \
  "v_cvt_pk_bf16_f32 v103, v46, v47\n\t" \
  "v_exp_f32 v50, v50\n\t" \
  "v_exp_f32 v51, v51\n\t" \
  "v_cvt_pk_bf16_f32 v96, v48, v49\n\t" \
  "v_mfma_f32_32x32x16_bf16 v[0:15], v[186:189], v[108:111], v[0:15]\n\t" \
  "v_exp_f32 v52, v52\n\t" \
  "v_exp_f32 v53, v53\n\t" \
  "v_cvt_pk_bf16_f32 v97, v50, v51\n\t" \
  "v_exp_f32 v54, v54\n\t" \
  "v_exp_f32 v55, v55\n\t" \
  "v_cvt_pk_bf16_f32 v98, v52, v53\n\t" \
  "v_mfma_f32_32x32x16_bf16 v[16:31], v[190:193], v[108:111], v[16:31]\n\t" \
  "v_exp_f32 v56, v56\n\t" \
  "v_exp_f32 v57, v57\n\t" \
  "v_cvt_pk_bf16_f32 v99, v54, v55\n\t" \
  "v_exp_f32 v58, v58\n\t" \
  "v_exp_f32 v59, v59\n\t" \
  "v_cvt_pk_bf16_f32 v104, v56, v57\n\t" \
  "v_mfma_f32_32x32x16_bf16 v[80:95], v[174:177], v[100:103], v[80:95]\n\t" \
  "v_exp_f32 v60, v60\n\t" \
  "v_exp_f32 v61, v61\n\t" \
  "v_cvt_pk_bf16_f32 v105, v58, v59\n\t" \
  "v_exp_f32 v62, v62\n\t" \
  "v_exp_f32 v63, v63\n\t" \
  "v_cvt_pk_bf16_f32 v106, v60, v61\n\t" \
  "v_mfma_f32_32x32x16_bf16 v[80:95], v[178:181], v[96:99], v[80:95]\n\t" \
  "v_exp_f32 v0, v0\n\t" \
  "v_exp_f32 v1, v1\n\t" \
  "v_cvt_pk_bf16_f32 v107, v62, v63\n\t" \
  "v_exp_f32 v2, v2\n\t" \
  "v_exp_f32 v3, v3\n\t" \
  "v_cvt_pk_bf16_f32 v96, v0, v1\n\t" \
  "v_mfma_f32_32x32x4_2b_bf16 v[32:63], v[194:195], v[118:119], 0\n\t" \
  "v_exp_f32 v4, v4\n\t" \
  "v_exp_f32 v5, v5\n\t" \
  "v_cvt_pk_bf16_f32 v97, v2, v3\n\t" \
  "v_exp_f32 v6, v6\n\t" \
  "v_exp_f32 v7, v7\n\t" \
  "v_cvt_pk_bf16_f32 v98, v4, v5\n\t" \
  "v_exp_f32 v8, v8\n\t" \
  "v_exp_f32 v9, v9\n\t" \
  "v_cvt_pk_bf16_f32 v99, v6, v7\n\t" \
  "v_exp_f32 v10, v10\n\t" \
  "v_exp_f32 v11, v11\n\t" \
  "v_cvt_pk_bf16_f32 v100, v8, v9\n\t" \
  "v_mfma_f32_32x32x16_bf16 v[80:95], v[182:185], v[104:107], v[80:95]\n\t" \
  "v_exp_f32 v12, v12\n\t" \
  "v_exp_f32 v13, v13\n\t" \
  "v_cvt_pk_bf16_f32 v101, v10, v11\n\t" \
  "v_exp_f32 v14, v14\n\t" \
  "v_exp_f32 v15, v15\n\t" \
  "v_cvt_pk_bf16_f32 v102, v12, v13\n\t" \
  "s_waitcnt vmcnt(4)\n\t" \
  "v_mfma_f32_32x32x16_bf16 v[64:79], v[196:199], v[96:99], v[64:79]\n\t" \
  "v_exp_f32 v16, v16\n\t" \
  "v_exp_f32 v17, v17\n\t" \
  "v_cvt_pk_bf16_f32 v103, v14, v15\n\t" \
  "v_exp_f32 v18, v18\n\t" \
  "v_exp_f32 v19, v19\n\t" \
  "v_cvt_pk_bf16_f32 v96, v16, v17\n\t" \
  "v_mfma_f32_32x32x16_bf16 v[32:47], v[186:189], v[112:115], v[32:47]\n\t" \
  "v_exp_f32 v20, v20\n\t" \
  "v_exp_f32 v21, v21\n\t" \
  "v_cvt_pk_bf16_f32 v97, v18, v19\n\t" \
  "v_exp_f32 v22, v22\n\t" \
  "v_exp_f32 v23, v23\n\t" \
  "v_cvt_pk_bf16_f32 v98, v20, v21\n\t" \
  "v_mfma_f32_32x32x16_bf16 v[48:63], v[190:193], v[112:115], v[48:63]\n\t" \
  "v_exp_f32 v24, v24\n\t" \
  "v_exp_f32 v25, v25\n\t" \
  "v_cvt_pk_bf16_f32 v99, v22, v23\n\t" \
  "v_exp_f32 v26, v26\n\t" \
  "v_exp_f32 v27, v27\n\t" \
  "v_cvt_pk_bf16_f32 v104, v24, v25\n\t" \
  "v_mfma_f32_32x32x16_bf16 v[64:79], v[200:203], v[100:103], v[64:79]\n\t" \
  "v_exp_f32 v28, v28\n\t" \
  "v_exp_f32 v29, v29\n\t" \
  "v_cvt_pk_bf16_f32 v105, v26, v27\n\t" \
  "v_exp_f32 v30, v30\n\t" \
  "v_exp_f32 v31, v31\n\t" \
  "v_cvt_pk_bf16_f32 v106, v28, v29\n\t" \
  "v_mfma_f32_32x32x16_bf16 v[64:79], v[204:207], v[96:99], v[64:79]\n\t" \
  "v_exp_f32 v32, v32\n\t" \
  "v_exp_f32 v33, v33\n\t" \
  "v_cvt_pk_bf16_f32 v107, v30, v31\n\t" \
  "v_exp_f32 v34, v34\n\t" \
  "v_exp_f32 v35, v35\n\t" \
  "v_cvt_pk_bf16_f32 v96, v32, v33\n\t" \
  "v_exp_f32 v36, v36\n\t" \
  "v_exp_f32 v37, v37\n\t" \
  "v_cvt_pk_bf16_f32 v97, v34, v35\n\t" \
  "v_exp_f32 v38, v38\n\t" \
  "v_exp_f32 v39, v39\n\t" \
  "v_cvt_pk_bf16_f32 v98, v36, v37\n\t" \
  "v_exp_f32 v40, v40\n\t" \
  "v_exp_f32 v41, v41\n\t" \
  "v_cvt_pk_bf16_f32 v99, v38, v39\n\t" \
  "v_exp_f32 v42, v42\n\t" \
  "v_exp_f32 v43, v43\n\t" \
  "v_cvt_pk_bf16_f32 v100, v40, v41\n\t" \
  "v_mfma_f32_32x32x16_bf16 v[64:79], v[208:211], v[104:107], v[64:79]\n\t" \
  "v_exp_f32 v44, v44\n\t" \
  "v_exp_f32 v45, v45\n\t" \
  "v_cvt_pk_bf16_f32 v101, v42, v43\n\t" \
  "v_exp_f32 v46, v46\n\t" \
  "v_exp_f32 v47, v47\n\t" \
  "v_cvt_pk_bf16_f32 v102, v44, v45\n\t" \
  "v_mfma_f32_32x32x16_bf16 v[80:95], v[196:199], v[96:99], v[80:95]\n\t" \
  "v_exp_f32 v48, v48\n\t" \
  "v_exp_f32 v49, v49\n\t" \
  "v_cvt_pk_bf16_f32 v103, v46, v47\n\t" \
  "v_exp_f32 v50, v50\n\t" \
  "v_exp_f32 v51, v51\n\t" \
  "v_cvt_pk_bf16_f32 v96, v48, v49\n\t" \
  "v_exp_f32 v52, v52\n\t" \
  "v_exp_f32 v53, v53\n\t" \
  "v_cvt_pk_bf16_f32 v97, v50, v51\n\t" \
  "v_exp_f32 v54, v54\n\t" \
  "v_exp_f32 v55, v55\n\t" \
  "v_cvt_pk_bf16_f32 v98, v52, v53\n\t" \
  "v_exp_f32 v56, v56\n\t" \
  "v_exp_f32 v57, v57\n\t" \
  "v_cvt_pk_bf16_f32 v99, v54, v55\n\t" \
  "v_exp_f32 v58, v58\n\t" \
  "v_exp_f32 v59, v59\n\t" \
  "v_cvt_pk_bf16_f32 v104, v56, v57\n\t" \
  "v_mfma_f32_32x32x16_bf16 v[80:95], v[200:203], v[100:103], v[80:95]\n\t" \
  "v_exp_f32 v60, v60\n\t" \
  "v_exp_f32 v61, v61\n\t" \
  "v_cvt_pk_bf16_f32 v105, v58, v59\n\t" \
  "v_exp_f32 v62, v62\n\t" \
  "v_exp_f32 v63, v63\n\t" \
  "v_cvt_pk_bf16_f32 v106, v60, v61\n\t" \
  "s_nop 0\n\t" \
  "v_cvt_pk_bf16_f32 v107, v62, v63\n\t" \
  "v_mfma_f32_32x32x16_bf16 v[80:95], v[204:207], v[96:99], v[80:95]\n\t" \
  "s_nop 1\n\t" \
  "v_mfma_f32_32x32x16_bf16 v[80:95], v[208:211], v[104:107], v[80:95]\n\t" \
  "9:\n\t" \
  "s_setprio 0\n\t" \
  "s_waitcnt vmcnt(0)\n\t" \
  "s_nop 9\n\t" \
  ""
#define ATT_CLOBBERS "v0", "v1", "v2", "v3", "v4", "v5", "v6", "v7", "v8", "v9", "v10", "v11", "v12", "v13", "v14", "v15", "v16", "v17", "v18", "v19", "v20", "v21", "v22", "v23", "v24", "v25", "v26", "v27", "v28", "v29", "v30", "v31", "v32", "v33", "v34", "v35", "v36", "v37", "v38", "v39", "v40", "v41", "v42", "v43", "v44", "v45", "v46", "v47", "v48", "v49", "v50", "v51", "v52", "v53", "v54", "v55", "v56", "v57", "v58", "v59", "v60", "v61", "v62", "v63", "v96", "v97", "v98", "v99", "v100", "v101", "v102", "v103", "v104", "v105", "v106", "v107", "v124", "v125", "v126", "v127", "v128", "v132", "v134", "v135", "v136", "v137", "v138", "v139", "v140", "v141", "v142", "v143", "v144", "v145", "v146", "v147", "v148", "v149", "v150", "v151", "v152", "v153", "v154", "v155", "v156", "v157", "v158", "v159", "v160", "v161", "v162", "v163", "v164", "v165", "v166", "v167", "v168", "v169", "v170", "v171", "v172", "v173", "v174", "v175", "v176", "v177", "v178", "v179", "v180", "v181", "v182", "v183", "v184", "v185"

DEV float colsum4(u32x2 a, u32x2 b, float cs) { return dot2(a[0], 0x3F803F80u, dot2(a[1], 0x3F803F80u, dot2(b[0], 0x3F803F80u, dot2(b[1], 0x3F803F80u, cs)))); }
DEV void put_frag(u32x2 a, u32x2 b, rsrc_t ws, u32 dst, u32 cdst, int lane) {
  st128<SC1>(u32x4{a[0], a[1], b[0], b[1]}, ws, lane * 16, dst);
  u32 x = asu(colsum4(a, b, 0.f)), y = x;
  swap32(x, y);
  st32<SC1>(asu(asf(x) + asf(y)), ws, lane < 32 ? lane * 4 : OOB, cdst);
}
DEV void aug_load(rsrc_t ws, u32 src, u32 sstep, int ntile, int t0, int lane, float (&p)[40]) {
  const int t = t0 + (lane >> 5);
  const u32 v = t < ntile ? t * 128 + (lane & 31) * 4 : OOB;
#pragma unroll
  for (int i = 0; i < 40; ++i) p[i] = asf(__builtin_amdgcn_raw_buffer_load_b32(ws, v, src + i * sstep, SC1));
  SBAR();
}
DEV void aug_store(rsrc_t ws, u32 dst, u32 lds, int ntile, int t0, int lane, float (&p)[40]) {
  const int t = t0 + (lane >> 5);
  float s0 = 0.f, s1 = 0.f, s2 = 0.f, s3 = 0.f;
#pragma unroll
  for (int i = 0; i < 40; i += 4) { s0 += p[i]; s1 += p[i + 1]; s2 += p[i + 2]; s3 += p[i + 3]; }
  const float cs = (s0 + s1) + (s2 + s3);
  const u32 hi = pk(cs, cs), lo = pk(cs - asf(hi << 16), 0.f), o = t < ntile ? t * 1024 + (lane & 31) * 16 : OOB;
  u32 z = 0;
  PIN(z);
  st128<SC1>(u32x4{hi, lo, z, z}, ws, o, dst);
  st128<SC1>(u32x4{z, z, z, z}, ws, o, dst + 512);
  if (lds != OOB) {
    *(LAS u32x4 *)(uintptr_t)(lds + o) = u32x4{hi, lo, z, z};
    *(LAS u32x4 *)(uintptr_t)(lds + o + 512) = u32x4{z, z, z, z};
  }
}
DEV void make_aug(rsrc_t ws, u32 src, u32 sstep, u32 dst, u32 lds, int ntile, int t0, int lane) {
  float p[40];
  aug_load(ws, src, sstep, ntile, t0, lane, p);
  aug_store(ws, dst, lds, ntile, t0, lane, p);
}
DEV void rest_load(const Params &p, int r, int c, int hw, int lane, u32x4 (&raw)[10]) {
  const int v = 4 * r + hw;
  const GAS char *base[10];
  u32 off[10];
  if (v < 40) {
#pragma unroll
    for (int i = 0; i < 10; ++i) {
      const u32 idx = (i < 7 ? i : 6) * 64 + lane, row = (idx * 2341) >> 16, ch = idx - row * 28;
      base[i] = (const GAS char *)(ch >= 14 ? p.wu : p.wg);
      off[i] = (16 * v + row) * (FF * 2) + ((27 * c) >> 2) * 32 + (ch >= 14 ? ch - 14 : ch) * 16;
    }
  } else {
    const int u = (c & 1) * 24 + v - 40;
#pragma unroll
    for (int k = 0; k < 2; ++k) {
      const int ks = (k && u >= 26) ? u : u + 48 * k;
      const bool is2 = ks < 20;
#pragma unroll
      for (int i = 0; i < 5; ++i) {
        const u32 idx = i * 64 + lane, rho = (idx * 205) >> 11, ch = idx - rho * 10;
        const u32 n = 4 * ks + 2 * (rho >> 4) + ((rho >> 2) & 1), cc = (n * 205) >> 10, j = n - 5 * cc, ii = (rho >> 3) & 1;
        const u32 tb = (u32)(0x1018140400ull >> (8 * j)) & 0xFFu;
        const u32 row2 = 40 * cc + tb + ii * (j < 4 ? 8 : 20) + (rho & 3), row4 = 32 * (ks - 20) + rho, m = is2 ? ~0u : 0u;
        base[5 * k + i] = (const GAS char *)(is2 ? p.wo : p.wd);
        off[5 * k + i] = ((row2 & m) | (row4 & ~m)) * 1280 + ch * 16 + (c >> 1) * 160;
      }
    }
  }
#pragma unroll
  for (int i = 0; i < 10; ++i) { base[i] += off[i]; PIN(base[i]); }
  BAR();
#pragma unroll
  for (int i = 0; i < 10; ++i) raw[i] = *(const GAS u32x4 *)base[i];
  SBAR();
}
DEV void rest_store(rsrc_t ws, u32 lw, int r, int c, int hw, int lane, u32x4 (&raw)[10]) {
  const int v = 4 * r + hw, q = (lane & 15) >> 2, pp = lane & 3, g = lane >> 4;
#pragma unroll
  for (int i = 0; i < 10; ++i) PIN(raw[i]);
  if (v < 40) {
#pragma unroll
    for (int i = 0; i < 7; ++i) *(LAS u32x4 *)(uintptr_t)(lw + i * 1024 + lane * 16) = raw[i];
    const u32 ra = lw + (4 * (lane >> 5) + q) * 448 + (g & 1) * 224 + pp * 8;
#pragma unroll
    for (int t = 0; t < 7; ++t) put_frag(ldtr(ra + t * 32), ldtr(ra + t * 32 + 8 * 448), ws, OFF_W3 + (c * 40 + v) * 7168 + t * 1024, OFF_C3 + (c * 40 + v) * 1024 + t * 128, lane);
  } else {
    const u32 ra = lw + (16 * (g >> 1) + 4 * (g & 1) + q) * 160 + pp * 8;
#pragma unroll
    for (int k = 0; k < 2; ++k) {
      const int ks = (c & 1) * 24 + v - 40 + 48 * k, kk = ks < 20 ? ks : ks - 20, nh = ks < 20 ? 10 : 13, kh = kk >= nh, u = kk - kh * nh;
      const u32 dst = (ks < 20 ? OFF_W2 + (c >> 1) * 5 * 20480 : OFF_W4 + (c >> 1) * 14 * 20480) + (kk < 26 ? (u >> 1) * 4 + 2 * kh + (u & 1) : kk + 2) * 5120;
      if (ks < 74) {
#pragma unroll
        for (int i = 0; i < 5; ++i) *(LAS u32x4 *)(uintptr_t)(lw + i * 1024 + lane * 16) = raw[5 * k + i];
#pragma unroll
        for (int i = 0; i < 5; ++i) {
          const u32x2 a = ldtr(ra + i * 32), b = ldtr(ra + i * 32 + 1280);
          st128<SC1>(u32x4{a[0], a[1], b[0], b[1]}, ws, lane * 16, dst + i * 1024);
        }
      }
    }
  }
}

#define SIGE 2
#define SIGA 1
#define SIG3 3
#define RP 2
#define T1A 32
#define T1B 48
#define NH0 22
#define HD0 28
#define HP4 1
#define SIGK2 1
#define SIGK4 1
#define MAP 1
#define G1B 0x111u
#define G1N 5
#define HP1 2
#define PF0 1
#define OAUX 0
#define P3 1
#define NHA 3
#define NHB 5
#define PFD 1
#define PFA -1
#define PFS 6
#define PACE 1
template <int SLEEP> DEV void fill_lin(rsrc_t ws, u32 lds, u32 src, int n, int lane) {
  const u32 v = lane * 16;
#pragma unroll 1
  for (int i = 0; i < n; ++i) {
    dma(lds + i * 1024, v, ws, src + i * 1024);
    if (SLEEP) __builtin_amdgcn_s_sleep(SLEEP);
  }
}
template <int SLEEP> DEV void fill_g1(rsrc_t ws, u32 L, int c, int hw, int lane, int s0, int s1) {
#pragma unroll 1
  for (int s = s0; s < s1; ++s) dman<4, SLEEP>(L + (s & 7) * 16384 + hw * 4096, lane * 16, ws, OFF_W1 + (c * 40 + 4 * s + hw) * 4096);
}
template <int SLEEP> DEV void fill_g4(rsrc_t ws, u32 L, int c, int hw, int lane, int s0, int s1) {
#pragma unroll 1
  for (int s = s0; s < s1; ++s)
    if (2 * s + (hw & 1) < 13) {
      const u32 d = L + ((6 + s) % 7) * 20480 + hw * 5120, g = OFF_W4 + (((c >> 1) * 14 + s) * 4 + hw) * 5120;
      dman<3, SLEEP>(d, lane * 16, ws, g);
      dman<2, SLEEP>(d + 3072, lane * 16, ws, g + 3072);
    }
}
template <int SLEEP> DEV void fill_g3(rsrc_t ws, u32 L, int c, int hw, int lane, int s0, int s1) {
#pragma unroll 1
  for (int s = s0; s < s1; ++s) {
    const u32 d = L + ((4 + s) % 5) * 28672 + hw * 7168, g = OFF_W3 + (c * 40 + 4 * s + hw) * 7168;
    dman<4, SLEEP>(d, lane * 16, ws, g);
    dman<3, SLEEP>(d + 4096, lane * 16, ws, g + 4096);
  }
}

DEV void prefill(rsrc_t ws, u32 o, int n, int lane) {
  u32 m = ~0u;
  PIN(m);
#pragma unroll 1
  for (int i = 0; i < n; ++i) st128<SC1>(u32x4{m, m, m, m}, ws, lane * 16, o + i * 1024);
}

DEV void touch1(u32 &d, u32 v, rsrc_t r, u32 s) { TOUCH(d, v, r, s); }

DEV void group_signal(unsigned char *ws, int ph, int first, int stride, int lane) {
  if (lane < 16) __hip_atomic_fetch_add((GAS u32 *)ws + ph * 8192 + (first + lane * stride) * 32, 1u, RLX_AGENT);
}
DEV void group_wait(unsigned char *ws, int ph, int id, u32 want = 16u) {
  const unsigned char *c = ws + ph * 32768 + id * 128;
  for (u32 spins = 0; spins < (1u << 21); ++spins) {
    u32 v;
    asm volatile("s_load_dword %0, %1, 0x0 glc\n\ts_waitcnt lgkmcnt(0)" : "=s"(v) : "s"(c) : "memory");
    if (v >= want) break;
  }
}
DEV void tag_wait(rsrc_t ws, u32 line, u32 tag, int lane) {
  for (u32 spins = 0; spins < (1u << 20); ++spins) {
    asm volatile("" ::: "memory");
    const u32x2 v = ld64<SC1>(ws, (lane & 15) * 8, line);
    if (__all(v[0] == tag && v[1] == ~tag)) break;
  }
  asm volatile("" ::: "memory");
}
DEV void ln_stats(float s1, float s2, float &mean, float &rs) {
  mean = s1 * (1.0f / DM);
  rs = __builtin_amdgcn_rsqf(fmaxf(__builtin_fmaf(-mean, mean, s2 * (1.0f / DM)), 0.f) + 1e-5f);
}

#define XPN 10
constexpr int XP = XPN;
constexpr int xissued(int s, int XW) { return XP + 2 * (s + 1) < 10 + (s + 1) ? XP + 2 * (s + 1) : 10 + (s + 1) + (s + 1) / 4 < s + 1 + XW ? 10 + (s + 1) + (s + 1) / 4 : s + 1 + XW; }
constexpr int RS = 10;
constexpr u32 L_STAT = L_SCR + 18432;
template <int NT, int NSLOT, int SLOT0, int XW, u32 BARS, u32 AUG, bool RAW, class EPI, class MID>
DEV void ln_gemm(rsrc_t ws, u32 L, u32 xs, u32 ss, int sb_, int lane, f32x16 (&acc)[NT], float &rs, EPI &&epi, MID &&mid SARG) {
  const u32 xv = lane * 16, sv = (lane & 31) * 8 + (lane >> 5) * 4096;
  STAMP(sb_ + 3);
#define XLD(i) ld128<SC1>(ws, xv + ((i) & 3) * 1024, xs + ((i) >> 2) * 4096)
  u32x4 x[41];
  constexpr u32 STG = RAW ? 32768 : NT * 4096;
  u32 wa = L + SLOT0 * STG + lane * 16;
  u32 wr[4], xa[4];
  if constexpr (RAW) {
    const u32 q = (lane & 15) >> 2, sh = 8 * (((lane >> 4) & 1) * 4 + (lane & 3)), h = lane >> 5, l31 = lane & 31;
    const u64 T[4] = {0x1B1A191817161514ull, 0x1E1E1D1C130E0904ull, 0x0D0C0B0A03020100ull, 0x1211100F08070605ull};
#pragma unroll
    for (int t = 0; t < 4; ++t) {
      wr[t] = L + (8 * h + q) * 256 + ((((u32)(T[t] >> sh) & 31u) << 3) ^ (q << 6));
      xa[t] = L + 16384 + xs + l31 * 128 + (((2 * t + h) ^ (l31 >> 1)) & 7) * 16;
    }
  } else {
#pragma unroll
    for (int i = 0; i < XP; ++i) {
      x[i] = XLD(i);
      SBAR();
      if (i < XP - 1) __builtin_amdgcn_s_sleep(1);
      SBAR();
    }
  }
  auto wfrag = [&](int st, int u, int t) __attribute__((always_inline)) -> u32x4 {
    if (RAW && st < RS) { const u32x2 a = ldtr(wr[t & 3] + u * 4096), b = ldtr(wr[t & 3] + u * 4096 + 1024); return u32x4{a[0], a[1], b[0], b[1]}; }
    else return lds128(wa + (u * NT + t) * 1024);
  };
  auto xfrag = [&](int st, int u) __attribute__((always_inline)) -> u32x4 {
    if (st < RS) return lds128(xa[u]);
    const u32x2 a = *(LAS u32x2 *)(uintptr_t)xa[u], b = *(LAS u32x2 *)(uintptr_t)wr[u];
    return u32x4{a[0], a[1], b[0], b[1]};
  };
  u32x4 wf[NT];
  if (BARS & 1) BAR();
  u32x2 ps[4];
  u64 chk = 0, chk0;
  u32 mx;
  if constexpr (RAW) x[0] = xfrag(0, 0);
#pragma unroll
  for (int t = 0; t < NT; ++t) { acc[t] = f32x16{}; wf[t] = wfrag(0, 0, t); }
  SBAR();
  if constexpr (!RAW) RECHECK(x[0], XLD(0));
  sfor<0, 36>([&](auto s_) __attribute__((always_inline)) {
    constexpr int s = decltype(s_)::v;
    constexpr int f0 = s ? xissued(s - 1, XW) : XP, f1 = xissued(s, XW);
    if (s == 1) STAMP(sb_);
    if (s == 21) STAMP(sb_ + 1);
    if ((s & 3) == 3) {
      if ((BARS >> ((s + 1) / 4)) & 1) BAR();
      constexpr u32 d = (SLOT0 + (s + 1) / 4) % NSLOT == 0 ? 0u - (NSLOT - 1) * STG : STG;
      if constexpr (RAW) {
#pragma unroll
        for (int t = 0; t < 4; ++t) {
          xa[t] += d;
          if ((s + 1) / 4 == RS) { xa[t] = (xa[t] & ~16u) + (lane >> 5) * 8 + ((lane >> 1) & 1) * 16; wr[t] = xa[t] ^ 16u; } else wr[t] += d;
          PIN(wr[t]); PIN(xa[t]);
        }
      }
      wa += d; PIN(wa);
    }
#pragma unroll
    for (int t = 0; t < NT; ++t) {
      acc[t] = mfma32(wf[t], x[s], acc[t]);
      SBAR();
      if constexpr (RAW) {
        if (t == 2) mid(s_);
        if (s < 35) { if (t == 0) x[s + 1] = xfrag((s + 1) / 4, (s + 1) & 3); } else x[36 + t] = xfrag(9, t);
      }
      if (s < 35) wf[t] = wfrag((s + 1) / 4, (s + 1) & 3, t);
      else if (t < 4) wf[t] = wfrag(9, t, 0);
      if constexpr (!RAW) {
        if constexpr (f0 < 40) { if (t == 0) x[f0] = XLD(f0); }
        if constexpr (f0 + 1 < f1 && f0 + 1 < 40) { if (t == 2) x[f0 + 1] = XLD(f0 + 1); }
        if constexpr (f0 >= 40 && f0 < 44) { if (t == 0) ps[f0 - 40] = ld64<SC1>(ws, sv + (f0 - 40) * 1024, ss); }
        if constexpr (s < 3) {
          if (t == 1) CHECK1(chk0, x[s + 1]);
          if (t == 2) CHECK2(chk0, x[s + 1], XLD(s + 1));
        }
        if (t == NT - 3) { mx = (s & 3) ? umax(umax(mx, x[s + 4][0]), x[s + 4][1]) : umax(x[s + 4][0], x[s + 4][1]); PIN(mx); }
        if (t == NT - 2) {
          mx = umax(umax(mx, x[s + 4][2]), x[s + 4][3]);
          if ((s & 3) == 3) { chk = __builtin_amdgcn_ballot_w64(mx == 0xFFFFFFFFu); SPIN(chk); } else PIN(mx);
        }
        if constexpr ((s & 3) == 3) {
          if (t == NT - 1) {
            if (__builtin_expect(chk != 0, 0)) { RETRY(x[s + 1], XLD(s + 1)); RETRY(x[s + 2], XLD(s + 2)); RETRY(x[s + 3], XLD(s + 3)); RETRY(x[s + 4], XLD(s + 4)); }
          }
        }
      }
      SBAR();
    }
  });
  STAMP(sb_ + 2);
  const u32 wb = L + AUG + lane * 16;
  u32x4 wg;
  if constexpr (RAW) ps[0] = *(LAS u32x2 *)(uintptr_t)(L + L_STAT + ss + (lane & 31) * 8); else wg = lds128(wb);
  sfor<0, NT + 1>([&](auto t_) __attribute__((always_inline)) {
    constexpr int t = decltype(t_)::v;
    sfor<0, 5>([&](auto u_) __attribute__((always_inline)) {
      constexpr int u = decltype(u_)::v;
      if constexpr (t < NT) {
        if constexpr (u < 4) acc[t] = mfma32(wf[u], x[36 + u], acc[t]); else acc[t] = mfma32(wg, x[40], acc[t]);
        SBAR();
        if constexpr (t + 1 < NT) {
          if constexpr (u < 4) wf[u] = wfrag(9, u, t + 1); else wg = lds128(wb + (t + 1) * 1024);
        }
        if constexpr (RAW && t == 0 && u == 2) { BAR(); wg = lds128(wb); }
        if constexpr (t == NT - 1 && u == 0 && (BARS >> 10)) BAR_LGKM();
        SBAR();
      }
      if constexpr (t == 0) {
        if constexpr (u == 0 && !RAW) {
          if (__builtin_expect(__any(unset(u32x4{umax(ps[0][0], ps[0][1]), umax(ps[1][0], ps[1][1]), umax(ps[2][0], ps[2][1]), umax(ps[3][0], ps[3][1])})), 0)) {
#pragma unroll
            for (int i = 0; i < 4; ++i) RETRY(ps[i], ld64<SC1>(ws, sv + i * 1024, ss));
          }
        }
        if constexpr (u == 1 && !RAW) {
          u32 a = asu((asf(ps[0][0]) + asf(ps[1][0])) + (asf(ps[2][0]) + asf(ps[3][0]))), b = asu((asf(ps[0][1]) + asf(ps[1][1])) + (asf(ps[2][1]) + asf(ps[3][1])));
          swap32(a, b);
          mx = asu(asf(a) + asf(b));
          PIN(mx);
        }
        if constexpr (u == 2) {
          u32 a, b;
          if constexpr (RAW) { a = ps[0][0]; b = ps[0][1]; } else { a = mx; b = mx; swap32(a, b); }
          float mean;
          ln_stats(asf(a), asf(b), mean, rs);
          const u32 hi = pk(-mean, 0.f), lo = pk(-mean - asf(hi << 16), 0.f);
          u32 z = 0;
          PIN(z);
          x[40] = u32x4{hi | (lo << 16), hi, z, z};
          PIN(x[40]); PIN(rs);
        }
      }
      if constexpr (t > 0) epi(IC<t - 1>{}, u_);
      SBAR();
    });
  });
}

template <int layer> DEV void g1_compute(const Params &p, rsrc_t ws, u32 L, int r, int c, int cw, int lane, u32 &td SARG) {
  FRESH(); SPIN(cw);
  const int h = lane >> 5, l31 = lane & 31;
  const int tile = 4 * r + cw;
  f32x16 acc[4];
  float rs;
  const u32 ar = OFF_AR + (layer * 16 + r) * ARENA;
  const u32 kv = OFF_KV + layer * KV_BYTES + (2 * c * 64 + tile) * REC;
  u32 vsw = L + 131072 + cw * 3072 + l31 * 96 + h * 8, vsm = vsw + h * 8;
  u32 vsr = L + 131072 + cw * 3072 + (4 * h + ((lane & 15) >> 2)) * 96 + ((lane >> 4) & 1) * 32 + (lane & 3) * 8;
  *(LAS u32x2 *)(uintptr_t)(vsw + 40 + h * 40) = u32x2{0x3F80u, 0u};
  PIN(vsm);
  u32 qsw = L + L_SCR + cw * 2560 + lane * 16, qst = L + L_SCR + cw * 2560 + h * 1280 + 1024 + l31 * 8;
  u32 vo = l31 <= 20 ? 1024 + h * 336 + l31 * 16 : OOB, ko = h * 64 * REC + 2368 + l31 * 8, mo = l31 == 0 ? h * 256 : OOB;
  u32 kl = L + L_KV + cw * REC + lane * 16, tl = L + L_KV + (4 * h + cw) * REC + 2368 + l31 * 8, vl = L + L_KV + cw * REC + (l31 <= 20 ? 1024 + h * 336 + l31 * 16 : 2624);
  const u32 vls = l31 <= 20 ? 672 : 0;
  PIN(kl); PIN(tl); PIN(vl);
  PIN(vsw); PIN(vsr); PIN(qsw); PIN(qst); PIN(vo); PIN(ko); PIN(mo);
  float vm = 0.f, na = 0.f, nb = 0.f;
  u32x2 ta[4], tb[4];
  auto vrow = [&](const f32x16 &a, int b, int piece) __attribute__((always_inline)) {
    const float v0 = a[b] * rs, v1 = a[b + 1] * rs, v2 = a[b + 2] * rs, v3 = a[b + 3] * rs;
    *(LAS u32x2 *)(uintptr_t)(piece == 2 ? vsm + 32 : vsw + piece * 16 + (piece > 2 ? 8 : 0)) = u32x2{pk(v0, v1), pk(v2, v3)};
    vm = fmaxf(fmaxf(vm, fabsf(v0)), fabsf(v1));
    vm = fmaxf(fmaxf(vm, fabsf(v2)), fabsf(v3));
    PIN(vm);
  };
  auto vout = [&](int i) __attribute__((always_inline)) {
    const u32x4 v = u32x4{ta[i][0], ta[i][1], tb[i][0], tb[i][1]};
    st128<SC1>(v, ws, vo + (i & 1) * 672, kv + (i >> 1) * 64 * REC);
    *(LAS u32x4 *)(uintptr_t)(vl + (i & 1) * vls + (i >> 1) * 4 * REC) = v;
  };
  ln_gemm<4, layer ? 8 : 4, 0, 16, layer ? G1B : 0x3FFu, layer ? 2 * 16384 : 65536, layer == 0>(ws, L, layer ? ar + A_X1 + cw * 40960 : cw * 4096, layer ? ar + A_S1 + cw * 256 : cw * 256, 32 + 8 * layer, lane, acc, rs, [&](auto t_, auto u_) __attribute__((always_inline)) {
    constexpr int t = decltype(t_)::v, u = decltype(u_)::v;
    const float qs_ = rs * (0.22360679774997896f * 1.4426950408889634f);
    if constexpr (t == 0) { if constexpr (u > 0) vrow(acc[0], 4 * (u - 1), u - 1); }
    if constexpr (t == 1) {
      if constexpr (u == 1) vrow(acc[1], 8, 4);
      if constexpr (u == 2) {
#pragma unroll
        for (int i = 0; i < 4; ++i) { ta[i] = ldtr(vsr + (i & 1) * 1536 + (i >> 1) * 48); tb[i] = ldtr(vsr + (i & 1) * 1536 + 768 + (i >> 1) * 48); }
      }
      if constexpr (u == 3) {
        const u32x2 kt = u32x2{pk(acc[1][4] * rs, acc[1][5] * rs), pk(acc[1][6] * rs, acc[1][7] * rs)};
        st64<SC1>(kt, ws, ko, kv);
        *(LAS u32x2 *)(uintptr_t)tl = kt;
        const float lo0 = asf(kt[0] << 16), hi0 = asf(kt[0] & 0xffff0000u), lo1 = asf(kt[1] << 16), hi1 = asf(kt[1] & 0xffff0000u);
        const float n = __builtin_fmaf(lo0, lo0, hi0 * hi0) + __builtin_fmaf(lo1, lo1, hi1 * hi1);
        na = h ? 0.f : n; nb = h ? n : 0.f;
        PIN(na); PIN(nb);
      }
      if constexpr (u == 4) { vout(0); *(LAS u32x2 *)(uintptr_t)qst = u32x2{pk(acc[1][0] * qs_, acc[1][1] * qs_), pk(acc[1][2] * qs_, acc[1][3] * qs_)}; }
    }
    if constexpr (t == 2) {
      if constexpr (u < 3) vout(u + 1);
      if constexpr (u == 3) {
        const u32x4 km = u32x4{pk(acc[2][8] * rs, acc[2][9] * rs), pk(acc[2][10] * rs, acc[2][11] * rs), pk(acc[2][12] * rs, acc[2][13] * rs), pk(acc[2][14] * rs, acc[2][15] * rs)};
        st128<SC1>(km, ws, lane * 16, kv);
        *(LAS u32x4 *)(uintptr_t)kl = km;
        ta[0] = u32x2{km[0], km[1]}; tb[0] = u32x2{km[2], km[3]};
      }
    }
    if constexpr (t == 3 && u == 0) {
      const u32x4 km = u32x4{pk(acc[3][8] * rs, acc[3][9] * rs), pk(acc[3][10] * rs, acc[3][11] * rs), pk(acc[3][12] * rs, acc[3][13] * rs), pk(acc[3][14] * rs, acc[3][15] * rs)};
      st128<SC1>(km, ws, lane * 16, kv + 64 * REC);
      *(LAS u32x4 *)(uintptr_t)(kl + 4 * REC) = km;
      na = dot2(ta[0][0], ta[0][0], dot2(ta[0][1], ta[0][1], dot2(tb[0][0], tb[0][0], dot2(tb[0][1], tb[0][1], na))));
      nb = dot2(km[0], km[0], dot2(km[1], km[1], dot2(km[2], km[2], dot2(km[3], km[3], nb))));
      if (!(vm <= 1.0e15f)) { na = __builtin_inff(); nb = na; }
      u32 ua = asu(na), ub = asu(nb);
      swap32(ua, ub);
      const float m = asf(ua) + asf(ub);
      st32<SC1>(halfmax_u(m == m ? asu(m) : 0x7F800000u), ws, mo, OFF_KM + layer * KM_BYTES + (2 * c * 64 + tile) * 4);
#pragma unroll
      for (int hd = 0; hd < 2; ++hd)
        *(LAS u32x4 *)(uintptr_t)(qsw + hd * 1280) = u32x4{pk(acc[2 + hd][0] * qs_, acc[2 + hd][1] * qs_), pk(acc[2 + hd][2] * qs_, acc[2 + hd][3] * qs_), pk(acc[2 + hd][4] * qs_, acc[2 + hd][5] * qs_), pk(acc[2 + hd][6] * qs_, acc[2 + hd][7] * qs_)};
    }
  }, [&](auto s_) __attribute__((always_inline)) {
    constexpr int s = decltype(s_)::v;
    const int id = 16 * r + c;
    if constexpr (s == PFA)
      touch1(td, lane < (cw < 2 ? 38 : 40) ? lane * 128 : OOB, mkrs(cw < 2 ? p.wqkv : p.x, cw < 2 ? DM * 3 * DM * 2 : SEQ * DM * 2), cw < 2 ? id * 9600 + cw * 4800 : id * 10240 + (cw & 1) * 5120);
    if constexpr (s == PFS || s == PFS + 4)
      touch1(td, lane < (cw ? 34 : 13) ? lane * 128 : OOB, mkrs(cw == 0 ? p.wo : cw == 1 ? p.wg : cw == 2 ? p.wu : p.wd, cw == 0 ? DM * DM * 2 : DM * FF * 2),
             id * (cw ? 8640 : 3200) + (s != PFS) * (cw ? 4320 : 1600));
  } SPASS);
}

#define EXP2(x) __builtin_amdgcn_exp2f(x)
#define ATT_ARGS u32x4 (&qm)[2], u32x4 (&kf)[2], u32x4 (&vf)[4], u32x2 (&qt)[2], u32x2 &tf
DEV void att_read(u32 L, int w, int lane, bool bar, ATT_ARGS) {
  PIN(lane); SPIN(L); SPIN(w);
  const int h = lane >> 5, l31 = lane & 31, hb = w & 1, qp = (w >> 1) & 1, kh = w >> 2;
  const u32 kb = L + L_KV + (4 * hb + 2 * kh) * REC, vb = kb + 1024 + h * 336 + (l31 < 20 ? l31 : 20) * 16;
  tf = *(LAS u32x2 *)(uintptr_t)(kb + h * REC + 2368 + l31 * 8);
#pragma unroll
  for (int i = 0; i < 2; ++i) {
    const u32 qs = L + L_SCR + (2 * qp + (i ^ kh)) * 2560 + hb * 1280;
    qt[i] = *(LAS u32x2 *)(uintptr_t)(qs + 1024 + l31 * 8);
    qm[i] = lds128(qs + lane * 16);
    kf[i] = lds128(kb + i * REC + lane * 16);
  }
#pragma unroll
  for (int i = 0; i < 4; ++i) vf[i] = lds128(vb + (i >> 1) * REC + (i & 1) * 672);
  if (bar) BAR_LGKM();
}
DEV void attention(unsigned char *wsp, rsrc_t ws, u32 L, int layer, int r, int c, int w, int lane, ATT_ARGS SARG) {
  FRESH(); SPIN(w);
  const int h = lane >> 5, l31 = lane & 31;
  const int hb = w & 1, qp = (w >> 1) & 1, kh = w >> 2, head = 2 * c + hb;
  const u32 kvb = OFF_KV + layer * KV_BYTES + head * 64 * REC;
  const u32 vK = lane * 16, vT = h * REC + 2368 + l31 * 8, vV = l31 <= 20 ? 1024 + h * 336 + l31 * 16 : OOB;
  float qn = 0.f;
#pragma unroll
  for (int i = 0; i < 2; ++i) {
    float n = 0.f;
#pragma unroll
    for (int j = 0; j < 4; ++j) n = dot2(qm[i][j], qm[i][j], n);
    u32 x = asu(n), y = asu(n);
    swap32(x, y);
    n = dot2(qt[i][0], qt[i][0], dot2(qt[i][1], qt[i][1], asf(x) + asf(y)));
    qn = fmaxf(qn, n);
    if (!(n == n)) qn = __builtin_inff();
  }
  STAMP(28 + layer * 2);
  float m0 = 0.f, m1 = 0.f;
  f32x16 o0, o1;
  const u32 srcA = OFF_W2 + (c >> 1) * 102400 + w * 12288, dstA = L + w * 12288;
  const u32 srcB = w ? OFF_W3 + c * 40 * 7168 + (w - 1) * 4096 : srcA + 98304, dstB = w ? L + 114688 + (w - 1) * 4096 : dstA + 98304;
  const u32 ps0 = OFF_AR + ((layer + 1) * 16 + r) * ARENA + (58 * c + ((29 * w) >> 2)) * 1024;
  u32 fast;
  {
    u32 psrc = ps0;
    u32 sk = kvb + ((4 * r + 4 + 30 * kh) & 63) * REC, sv, cnt, tmp, tmp2, dsrc = kvb, ddst = L + L_SCR + 16384, pf = kvb;
    asm volatile(ATT_ASM
                 : "={v[64:79]}"(o0), "={v[80:95]}"(o1), "+{v[186:189]}"(kf[0]), "+{v[190:193]}"(kf[1]), "+{v[194:195]}"(tf), "+{v[196:199]}"(vf[0]), "+{v[200:203]}"(vf[1]),
                   "+{v[204:207]}"(vf[2]), "+{v[208:211]}"(vf[3]), [sk] "+&s"(sk), [sv] "=&s"(sv), [cnt] "=&s"(cnt), [tmp] "=&s"(tmp), [tmp2] "=&s"(tmp2), [dsrc] "+&s"(dsrc),
                   [ddst] "+&s"(ddst), [pf] "+&s"(pf), [psrc] "+&s"(psrc), [fast] "=&s"(fast)
                 : "{v[108:111]}"(qm[0]), "{v[112:115]}"(qm[1]), "{v[116:117]}"(qt[0]), "{v[118:119]}"(qt[1]), "{v120}"(vK), "{v121}"(vT), "{v122}"(vV), "{v123}"(qn), "{v129}"(lane * 4),
                   "{v130}"(lane < 19 ? lane * 128 : OOB), "{v131}"(lane < 16 ? (u32)(16 * lane + c) * 128 : OOB),
                   "{v133}"((layer == 0 && kh && lane < 32) ? (u32)(16 * (lane & 15) + (c ^ (lane >> 4))) * 128 : OOB), [ws] "s"(ws), [kmo] "s"(OFF_KM + layer * KM_BYTES + head * 256),
                   [kbeg] "s"(kvb), [kend] "s"(kvb + 64 * REC), [dj0] "s"(srcA - kvb), [dj1] "s"(srcB - srcA - 8192), [dj2] "s"(dstB - dstA - 8192), [dj3] "s"(w * 12288 - L_SCR - 16384),
                   [pj] "s"(OFF_W3 + c * 40 * 7168 + 28672 + ((r & (MAP ? 3 : 1)) * 8 + w) * 7168 - kvb - 2432), [wat] "s"(layer * 96 + 3), [late] "s"(kh), [sig] "s"(layer * 5 * 32768),
                   [sig2] "s"(10 * 32768), [cptr] "s"(wsp + layer * 5 * 32768 + (16 * r + c) * 128)
                 : ATT_CLOBBERS, "vcc", "scc", "memory");
  }
  if (__builtin_expect(!fast, 0)) {
    const f32x16 Z = f32x16{};
    o0 = Z; o1 = Z;
    if (layer == 0) group_wait(wsp, 10, 16 * r + c, 128u);
    fill_lin<0>(ws, dstA, srcA, 12, lane);
    fill_lin<0>(ws, dstB, srcB, 4, lane);
    prefill(ws, ps0, 8, lane);
    m0 = m1 = -3.0e38f;
#pragma unroll 1
    for (int t = 0; t < 32; ++t) {
      const u32 so = kvb + (32 * kh + t) * REC;
      const u32x2 T2 = ld64<SC1>(ws, h ? OOB : vT, so);
      const u32x4 K = ld128<SC1>(ws, vK, so), T = u32x4{T2[0], T2[1], 0u, 0u}, V0 = ld128<SC1>(ws, vV, so), V1 = ld128<SC1>(ws, vV + 672, so);
#pragma unroll
      for (int i = 0; i < 2; ++i) {
        f32x16 s = mfma32(K, qm[i], Z);
        s = mfma32(T, u32x4{qt[i][0], qt[i][1], 0u, 0u}, s);
        float mx = s[0];
#pragma unroll
        for (int j = 1; j < 16; ++j) mx = fmaxf(mx, s[j]);
        u32 a = asu(mx), b = asu(mx);
        swap32(a, b);
        float &m = i ? m1 : m0;
        f32x16 &o = i ? o1 : o0;
        const float mn = fmaxf(m, fmaxf(asf(a), asf(b)));
        const float al = EXP2(m - mn);
        m = mn;
#pragma unroll
        for (int j = 0; j < 16; ++j) { s[j] = EXP2(s[j] - mn); o[j] *= al; }
        o = mfma32(V0, u32x4{pk(s[0], s[1]), pk(s[2], s[3]), pk(s[4], s[5]), pk(s[6], s[7])}, o);
        o = mfma32(V1, u32x4{pk(s[8], s[9]), pk(s[10], s[11]), pk(s[12], s[13]), pk(s[14], s[15])}, o);
      }
    }
    asm volatile("s_waitcnt vmcnt(0)" ::: "memory");
  }
  STAMP(29 + layer * 2);
  if (SIGA == 2 && w == 7) group_signal(wsp, layer * 5 + 1, 16 * r, 1, lane);
  f32x16 mine = o0, other = o1;
  const float mmine = m0;
  other[9] = h ? m1 : other[9];
  const u32 ca = L + L_GAP + (w & 3) * 3072 + lane * 16, cb = L + L_SCR + (w & 3) * 3072 + lane * 16, cs = w < 4 ? ca : cb, cr = w < 4 ? cb : ca;
  const u32 ct = L + L_SCR + 12288 + (w >> 1) * 1024 + lane * 16;
#pragma unroll
  for (int j = 0; j < 3; ++j) *(LAS f32x4 *)(uintptr_t)(cs + j * 1024) = f32x4{other[4 * j], other[4 * j + 1], other[4 * j + 2], other[4 * j + 3]};
  if (hb) *(LAS f32x4 *)(uintptr_t)ct = f32x4{mine[8], h ? mmine : mine[9], mine[10], mine[11]};
  const int token = 32 * (2 * qp + kh) + l31;
  const u32 ob = OFF_AR + (layer * 16 + r) * ARENA + A_O;
  u32 tb = (token >> 4) * 20480 + (token & 15) * 16;
  u32 o0v = tb + (5 * c + 2 * hb + h) * 256, o1v = (h || hb) ? OOB : tb + (5 * c + 4) * 256;
  PIN(o0v); PIN(o1v);
  BAR_LGKM();
  if (SIGA == 1 && w == 0) group_signal(wsp, layer * 5 + 1, 16 * r, 1, lane);
  f32x4 in[3], ta, tb2;
#pragma unroll
  for (int j = 0; j < 3; ++j) in[j] = *(LAS f32x4 *)(uintptr_t)(cr + j * 1024);
  ta = *(LAS f32x4 *)(uintptr_t)ct;
  tb2 = *(LAS f32x4 *)(uintptr_t)(cr + (hb ? 0 : 3072) + 2048);
  float fa = 1.f, fb = 1.f, ga = 1.f, gb = 1.f;
  if (__builtin_expect(!fast || __any(h && (ta[1] != 0.f || tb2[1] != 0.f)), 0)) {
    u32 a = asu(in[2][1]), b = a;
    swap32(a, b);
    const float mo = asf(b), mn = fmaxf(mo, mmine);
    fa = EXP2(mmine - mn);
    fb = EXP2(mo - mn);
    a = asu(ta[1]); b = a;
    swap32(a, b);
    u32 e = asu(tb2[1]), f = e;
    swap32(e, f);
    const float m2 = asf(b), m3 = asf(f), mm = fmaxf(m2, m3);
    ga = EXP2(m2 - mm);
    gb = EXP2(m3 - mm);
#pragma unroll
    for (int j = 0; j < 4; ++j) { in[0][j] *= fb; in[1][j] *= fb; in[2][j] *= fb; ta[j] *= ga; tb2[j] *= gb; }
#pragma unroll
    for (int j = 0; j < 12; ++j) mine[j] *= fa;
  }
  float o[12];
#pragma unroll
  for (int j = 0; j < 12; ++j) o[j] = mine[j] + in[j >> 2][j & 3];
  ta += tb2;
  u32 la = asu(o[8]), lb = la, lc = asu(ta[0]), ld = lc;
  swap32(la, lb);
  swap32(lc, ld);
  const float inv = __builtin_amdgcn_rcpf(asf(lb)), inv1 = __builtin_amdgcn_rcpf(asf(ld));
  st128<SC1>(u32x4{pk(o[0] * inv, o[1] * inv), pk(o[2] * inv, o[3] * inv), pk(o[4] * inv, o[5] * inv), pk(o[6] * inv, o[7] * inv)}, ws, o0v, ob);
  st128<SC1>(u32x4{pk(o[8] * inv, o[9] * inv), pk(o[10] * inv, o[11] * inv), pk(ta[0] * inv1, ta[1] * inv1), pk(ta[2] * inv1, ta[3] * inv1)}, ws, o1v, ob);
  if (SIGA == 0 && w == 0) group_signal(wsp, layer * 5 + 1, 16 * r, 1, lane);
}

constexpr u32 L_PART[4] = {L_SCR, 122880, 0, 20480};
template <int NK, int SLOT0, bool DOWN>
DEV void gd_compute(const Params &p, rsrc_t ws, u32 L, int layer, int r, int c, int cw, int lane, u32 (&res)[10] SARG) {
  FRESH(); SPIN(cw);
  const int th = cw >> 1, kh = cw & 1, g = lane >> 4, l15 = lane & 15;
  const int t0 = 64 * (c & 1) + 32 * th;
  const int tok = t0 + 16 * kh + l15, ct = c >> 1;
  const u32 ar = OFF_AR + ((layer + 1) * 16 + r) * ARENA;
  const int sb_ = 48 + 4 * (2 * layer + DOWN); (void)sb_;
  constexpr int NKS = DOWN ? 54 : 20, AP = DOWN ? 3 : 4, WIN = DOWN ? 6 : 10, GROW = DOWN ? 2 : 1;
  const u32 ab = (DOWN ? ar + A_H : ar - 16 * ARENA + A_O) + ((t0 >> 4) * NKS + kh * NK) * 1024;
  const u32 as0 = ab + kh * NKS * 1024, as1 = ab + (kh ^ 1) * NKS * 1024;
  const u32 av = lane * 16;
  STAMP(sb_ + 3);
#define ALD0(i) ld128<SC1>(ws, av + ((i) & 3) * 1024, as0 + ((i) >> 2) * 4096)
#define ALD1(i) ld128<SC1>(ws, av + ((i) & 3) * 1024, as1 + ((i) >> 2) * 4096)
  u32x4 a0[NK], a1[NK];
#pragma unroll
  for (int i = 0; i < AP; ++i) {
    a0[i] = ALD0(i);
    SBAR(); __builtin_amdgcn_s_sleep(1); SBAR();
    a1[i] = ALD1(i);
    SBAR(); if (i < AP - 1) __builtin_amdgcn_s_sleep(1); SBAR();
  }
  if (!DOWN) BAR();
  u32x2 p0, p1;
  u64 chk;
  u32 wa = L + SLOT0 * 20480 + kh * 10240 + lane * 16;
  u32 xs = L + (DOWN ? 40960 : L_SCR) + cw * 5120 + lane * 16, xr = L + (DOWN ? 40960 : L_SCR) + (cw ^ 1) * 5120 + lane * 16;
  u32 xp = L + (cw == 0 ? L_PART[0] : cw == 1 ? L_PART[1] : cw == 2 ? L_PART[2] : L_PART[3]) + lane * 16;
  PIN(xp);
  PIN(xs); PIN(xr);
  f32x4 acc[2][5];
  u32x4 wf[5], wl[5];
#pragma unroll
  for (int j = 0; j < 5; ++j) { acc[0][j] = f32x4{}; acc[1][j] = f32x4{}; wf[j] = lds128(wa + j * 1024); }
  SBAR();
  RECHECK(a0[0], ALD0(0));
  RECHECK(a1[0], ALD1(0));
  sfor<0, NK - 1>([&](auto s_) __attribute__((always_inline)) {
    constexpr int s = decltype(s_)::v;
    constexpr int f0 = AP + s + s / GROW < s + WIN ? AP + s + s / GROW : s + WIN, f1 = AP + s + 1 + (s + 1) / GROW < s + 1 + WIN ? AP + s + 1 + (s + 1) / GROW : s + 1 + WIN;
    if (s == 1) STAMP(sb_);
    if (s == NK / 2) STAMP(sb_ + 1);
    if (SIGE == 3 && s == NK - 1 - (DOWN ? SIGK4 : SIGK2) && cw == 0 && !(DOWN && layer == 1)) group_signal(p.ws, layer * 5 + (DOWN ? 4 : 2), 16 * r, 1, lane);
    if (s & 1) {
      if (DOWN && (((s + 1) / 2) & 1)) BAR();
      if ((SLOT0 + (s + 1) / 2) % 7 == 0) wa -= 6 * 20480; else wa += 20480;
      PIN(wa);
    }
#pragma unroll
    for (int j = 0; j < 5; ++j) {
      acc[1][j] = mfma16(wf[j], a1[s], acc[1][j]);
      SBAR();
      if constexpr (f0 < NK) { if (j == 0) a0[f0] = ALD0(f0); if (j == 1) a1[f0] = ALD1(f0); }
      if constexpr (f0 + 1 < f1 && f0 + 1 < NK) { if (j == 2) a0[f0 + 1] = ALD0(f0 + 1); if (j == 3) a1[f0 + 1] = ALD1(f0 + 1); }
      if constexpr (DOWN && s == 8) { if (j == 3) p0 = ld64<SC1>(ws, tok * 8 + g * 2048, ar + A_S2); if (j == 4) p1 = ld64<SC1>(ws, tok * 8 + g * 2048 + 1024, ar + A_S2); }

      if (j == 3) { chk = __builtin_amdgcn_ballot_w64(unset2(a0[s + 1], a1[s + 1])); SPIN(chk); }
      if (j == 4) { if (__builtin_expect(chk != 0, 0)) { RETRY(a0[s + 1], ALD0(s + 1)); RETRY(a1[s + 1], ALD1(s + 1)); } }
      SBAR();
      acc[0][j] = mfma16(wf[j], a0[s], acc[0][j]);
      SBAR();
      if constexpr (s + 2 < NK) wf[j] = lds128(wa + ((s + 1) & 1) * 5120 + j * 1024); else wl[j] = lds128(wa + ((s + 1) & 1) * 5120 + j * 1024);
      SBAR();
    }
  });
  STAMP(sb_ + 2);
  if (SIGE == 2 && cw == 0 && !(DOWN && layer == 1)) group_signal(p.ws, layer * 5 + (DOWN ? 4 : 2), 16 * r, 1, lane);
#pragma unroll
  for (int j = 0; j < 5; ++j) acc[1][j] = mfma16(wl[j], a1[NK - 1], acc[1][j]);
  SBAR();
#pragma unroll
  for (int j = 0; j < 5; ++j) {
    acc[0][j] = mfma16(wl[j], a0[NK - 1], acc[0][j]);
    SBAR();
    *(LAS f32x4 *)(uintptr_t)(xs + j * 1024) = acc[1][j];
    SBAR();
  }
  float sc = 1.f;
  if (DOWN) {
    u32 a = asu(asf(p0[0]) + asf(p1[0])), b = asu(asf(p0[1]) + asf(p1[1]));
    swap32(a, b);
    a = asu(asf(a) + asf(b)); b = a;
    swap16(a, b);
    a = asu(asf(a) + asf(b)); b = a;
    swap32(a, b);
    float mean;
    ln_stats(asf(a), asf(b), mean, sc);
    sc *= sc;
  }
  f32x4 rf[5];
#pragma unroll
  for (int j = 0; j < 5; ++j) {
    rf[j] = f32x4{asf(res[2 * j] << 16), asf(res[2 * j] & 0xffff0000u), asf(res[2 * j + 1] << 16), asf(res[2 * j + 1] & 0xffff0000u)};
    rf[j] = DOWN ? acc[0][j] * sc + rf[j] : acc[0][j] + rf[j];
    PIN(rf[j]);
  }
  BAR_LGKM();
  if (SIGE == 1 && cw == 0 && !(DOWN && layer == 1)) group_signal(p.ws, layer * 5 + (DOWN ? 4 : 2), 16 * r, 1, lane);
  f32x4 in[5];
#pragma unroll
  for (int j = 0; j < 5; ++j) in[j] = *(LAS f32x4 *)(uintptr_t)(xr + j * 1024);
  if (DOWN) {
    f32x4 q[4][5];
#pragma unroll
    for (int k = 0; k < 4; ++k)
#pragma unroll
      for (int j = 0; j < 5; ++j) q[k][j] = *(LAS f32x4 *)(uintptr_t)(xp + k * 5120 + j * 1024);
    SBAR();
#pragma unroll
    for (int j = 0; j < 5; ++j) in[j] = (in[j] + q[0][j]) + q[1][j] + (q[2][j] + q[3][j]);
    BAR();
  }
  SBAR();
#pragma unroll
  for (int j = 0; j < 5; ++j) {
    const f32x4 v = DOWN ? in[j] * sc + rf[j] : in[j] + rf[j];
    res[2 * j] = pk(v[0], v[1]);
    res[2 * j + 1] = pk(v[2], v[3]);
  }
  if (DOWN && layer == 1) {
    const rsrc_t ro = mkrs(p.out, SEQ * DM * 2);
    const u32 oo = tok * (DM * 2) + (g & 1) * 32 + (g >> 1) * 16, os = r * (128 * DM * 2) + ct * 160;
#pragma unroll
    for (int j = 0; j < 4; j += 2) {
      u32 a0_ = res[2 * j], a1_ = res[2 * j + 1], b0_ = res[2 * j + 2], b1_ = res[2 * j + 3];
      swap16(a0_, b0_); swap16(a1_, b1_);
      st128<OAUX>(u32x4{a0_, a1_, b0_, b1_}, ro, oo + j * 32, os);
    }
    u32 a0_ = res[8], b0_ = res[8], a1_ = res[9], b1_ = res[9];
    swap16(a0_, b0_); swap16(a1_, b1_);
    st128<OAUX>(u32x4{a0_, a1_, b0_, b1_}, ro, (g & 1) ? OOB : oo + 128, os);
  } else {
    const u32 xb = ar + (DOWN ? A_X1 : A_X2) + ((t0 >> 5) * 40 + 5 * ct) * 1024;
    const u32 xo = (g & 1) * 512 + (tok & 31) * 16;
#pragma unroll
    for (int j = 0; j < 4; j += 2) {
      u32 a0_ = res[2 * j], a1_ = res[2 * j + 1], b0_ = res[2 * j + 2], b1_ = res[2 * j + 3];
      swap32(a0_, b0_); swap32(a1_, b1_);
      st128<SC1>(u32x4{a0_, a1_, b0_, b1_}, ws, xo + (j + (g >> 1)) * 1024, xb);
    }
    {
      u32 a0_ = res[8], b0_ = res[8], a1_ = res[9], b1_ = res[9];
      swap32(a0_, b0_); swap32(a1_, b1_);
      st128<SC1>(u32x4{a0_, a1_, b0_, b1_}, ws, lane < 32 ? xo + 4096 : OOB, xb);
    }
    if (SIGE == 0 && cw == 0) group_signal(p.ws, layer * 5 + (DOWN ? 4 : 2), 16 * r, 1, lane);
    float s1 = 0.f, s2 = 0.f;
#pragma unroll
    for (int j = 0; j < 10; ++j) { s1 = dot2(res[j], 0x3F803F80u, s1); s2 = dot2(res[j], res[j], s2); }
    u32 a = asu(s1), b = asu(s2);
    swap32(a, b);
    a = asu(asf(a) + asf(b)); b = a;
    swap16(a, b);
    a = asu(asf(a) + asf(b)); b = a;
    swap32(a, b);
    st64<SC1>(u32x2{a, b}, ws, lane < 16 ? tok * 8 + ct * 1024 : OOB, ar + (DOWN ? A_S1 : A_S2));
  }
}

DEV void down_helper(rsrc_t ws, u32 L, int layer, int r, int c, int hw, int lane, u32x4 (&hold)[35], u32 hs SARG) {
  const u32 ab = OFF_AR + ((layer + 1) * 16 + r) * ARENA + A_H + ((c & 1) * 4 * 54 + 26 + 7 * hw) * 1024, av = lane * 16;
  const int sb_ = 52; (void)sb_;
#define HAL(i) ld128<SC1>(ws, av + (((i) % 7) & 3) * 1024, ab + ((i) / 7) * 55296 + (((i) % 7) >> 2) * 4096)
#define HLD(i) hold[i] = ld128<SC1>(ws, av + ((i) & 3) * 1024, hs + ((i) >> 2) * 4096)
  u32x4 a[28];

#pragma unroll
  for (int i = 0; i < 7; ++i) { a[i] = HAL(i); SBAR(); __builtin_amdgcn_s_sleep(1); SBAR(); }
  u32 pa = L + hw * 5120 + lane * 16;
  PIN(pa);

  VM_BAR(7);
  u32 td = 0;
  const u32 tv = (layer || lane >= T1A) ? OOB : lane * 128, t1 = OFF_W1 + c * 40 * 4096 + ((r & (MAP ? 3 : 1)) * 4 + hw) * (T1A * 128);
  __builtin_amdgcn_s_setprio(HP4);
  RECHECK(a[0], HAL(0));
  f32x4 acc[2][5];
  u64 chk;
  sfor<0, 28>([&](auto n_) __attribute__((always_inline)) {
    constexpr int n = decltype(n_)::v, t = n / 7, i = n % 7;

    if (n == 14 || n == 20) BAR();
#pragma unroll
    for (int j = 0; j < 5; ++j) {
      acc[t & 1][j] = mfma16(hold[5 * i + j], a[n], i ? acc[t & 1][j] : f32x4{});
      SBAR();
      if constexpr (n + 7 < 28) { if (j == 0) a[n + 7] = HAL(n + 7); }
      if constexpr (t > 0 && i >= 1 && i < 6) { if (j == 1) *(LAS f32x4 *)(uintptr_t)(pa + L_PART[t - 1] + (i - 1) * 1024) = acc[(t - 1) & 1][i - 1]; }
      if constexpr (n + 1 < 28) {
        if (j == 2) CHECK1(chk, a[n + 1]);
        if (j == 4) CHECK2(chk, a[n + 1], HAL(n + 1));
      }
      SBAR();
    }
  });
#pragma unroll
  for (int j = 0; j < 5; ++j) *(LAS f32x4 *)(uintptr_t)(pa + L_PART[3] + j * 1024) = acc[1][j];
  __builtin_amdgcn_s_setprio(0);
  touch1(td, tv, ws, t1);

  BAR_LGKM();
  BAR();
  if (layer == 0) {
    if (G1B == 0x111u) { fill_g1<PACE>(ws, L, c, hw, lane, 0, 8); VM_BAR(16); }
    else if (G1B == 0x115u) { fill_g1<PACE>(ws, L, c, hw, lane, 0, 5); VM_BAR(12); fill_g1<0>(ws, L, c, hw, lane, 5, 8); VM_BAR(16); }
    else { fill_g1<PACE>(ws, L, c, hw, lane, 0, G1N); asm volatile("s_waitcnt vmcnt(%0)\n\ts_barrier" ::"i"(4 * G1N - 4) : "memory");
           fill_g1<0>(ws, L, c, hw, lane, G1N, 8); VM_BAR(24); VM_BAR(20); VM_BAR(16); }
  }
  else asm volatile("s_waitcnt vmcnt(0)" ::: "memory");
  TOUCH_DONE(td);
}

DEV void g3_compute(unsigned char *wsp, rsrc_t ws, u32 L, int layer, int r, int c, int cw, int lane SARG) {
  FRESH(); SPIN(cw);
  f32x16 acc[7];
  float rs;
  const u32 ar = OFF_AR + ((layer + 1) * 16 + r) * ARENA;
  const int f0 = (27 * c) >> 2, nt = ((27 * (c + 1)) >> 2) - f0;
  const int tok = cw * 32 + (lane & 31);
  const u32 hb = ar + A_H + f0 * 512;
  u32 hv = (tok >> 4) * (54 * 1024) + (lane >> 5) * 256 + (tok & 15) * 16;
  u32 hv6 = nt == 7 ? hv : OOB;
  PIN(hv); PIN(hv6);
  u32x4 o;
  ln_gemm<7, 5, 4, 13, 0x7FEu, L_SCR, false>(ws, L, ar + A_X2 + cw * 40960, ar + A_S2 + cw * 256, 36 + 8 * layer, lane, acc, rs, [&](auto t_, auto u_) __attribute__((always_inline)) {
    constexpr int t = decltype(t_)::v, u = decltype(u_)::v;
    if constexpr (u > 0) {
      float a = fmaxf(acc[t][2 * u - 2], 0.f) * acc[t][2 * u + 6], b = fmaxf(acc[t][2 * u - 1], 0.f) * acc[t][2 * u + 7];
      PIN(a);
      o[u - 1] = pk(a, b);
      PIN(o);
    }
    if constexpr (u == 4) st128<SC1>(o, ws, t < 6 ? hv : hv6, hb + t * 512);
    if constexpr (u == 4 && t == SIG3) { if (cw == 0) group_signal(wsp, layer * 5 + 3, 16 * r, 1, lane); }
  }, [](auto) {} SPASS);
}

DEV u32 g1_raw_helper(const Params &p, rsrc_t ws, u32 L, int r, int c, int hw, int lane SARG) {
  const rsrc_t rw = mkrs(p.wqkv, DM * 3 * DM * 2), rx = mkrs(p.x, SEQ * DM * 2);
  const int id = 16 * r + c;
  const u32 code = (lane & 15) ^ ((lane >> 4) << 2);
  const u32 vw = code < 15 ? (lane >> 4) * 3840 + ((code * 52) >> 8) * 1200 + code * 16 + c * 80 : OOB;
  const u32 vx0 = (lane >> 3) * 1280 + ((lane & 7) ^ (lane >> 4)) * 16, vx1 = vx0 ^ 64u;
  f32x4 s1 = f32x4{}, s2 = f32x4{}, cs = f32x4{};
  s16x4 ones = s16x4{0x3F80, 0x3F80, 0x3F80, 0x3F80};
  PIN(ones);
  const u32 xb = L + 16384 + hw * 4096 + (lane & 31) * 128, xk = (((lane >> 5) ^ ((lane & 31) >> 1)) & 7) * 16;
  const u64 T = hw == 0 ? 0x1B1A191817161514ull : hw == 1 ? 0x1E1E1D1C130E0904ull : hw == 2 ? 0x0D0C0B0A03020100ull : 0x1211100F08070605ull;
  const u32 q = (lane & 15) >> 2;
  const u32 wb = L + (4 * (lane >> 5) + q) * 256 + ((((u32)(T >> (8 * (((lane >> 4) & 1) * 4 + (lane & 3)))) & 31u) << 3) ^ (q << 6));
  auto step = [&](int j, int k, int m) __attribute__((always_inline)) {
    u32x4 f[4];
    u32x2 wa[4], wc[4];
    if (k >= 0) {
#pragma unroll
      for (int u = 0; u < 4; ++u) f[u] = lds128(xb + (k & 3) * 32768 + (xk ^ (u * 32)));
    }
    if (m >= 0) {
#pragma unroll
      for (int u = 0; u < 4; ++u) { wa[u] = ldtr(wb + (m & 3) * 32768 + u * 4096); wc[u] = ldtr(wb + (m & 3) * 32768 + u * 4096 + 2048); }
    }
#pragma unroll
    for (int i = 0; i < 8; ++i) {
      if (j >= 0) {
        if (i & 1) dma(L + (j & 3) * 32768 + 16384 + hw * 4096 + (i >> 1) * 1024, (i & 2) ? vx1 : vx0, rx, (128 * r + 32 * hw + 8 * (i >> 1)) * 1280 + 128 * j);
        else dma(L + (j & 3) * 32768 + hw * 4096 + (i >> 1) * 1024, vw, rw, (64 * j + 16 * hw + 4 * (i >> 1)) * 3840);
      }
      if (k >= 0) {
        const s16x4 d = __builtin_bit_cast(s16x4, u32x2{f[i >> 1][2 * (i & 1)], f[i >> 1][2 * (i & 1) + 1]});
        s1 = __builtin_amdgcn_mfma_f32_4x4x4bf16_1k(ones, d, s1, 0, 0, 0);
        s2 = __builtin_amdgcn_mfma_f32_4x4x4bf16_1k(d, d, s2, 0, 0, 0);
        PIN(s1); PIN(s2);
      }
      if (m >= 0) {
        cs = __builtin_amdgcn_mfma_f32_4x4x4bf16_1k(ones, __builtin_bit_cast(s16x4, (i & 1) ? wc[i >> 1] : wa[i >> 1]), cs, 0, 0, 0);
        PIN(cs);
      }
      SBAR();
    }
    if (m >= 0) {
      const int u = r & 3;
      const u32x2 a = u == 0 ? wa[0] : u == 1 ? wa[1] : u == 2 ? wa[2] : wa[3], b = u == 0 ? wc[0] : u == 1 ? wc[1] : u == 2 ? wc[2] : wc[3];
      st128<SC1>(u32x4{a[0], a[1], b[0], b[1]}, ws, (m & 3) == (r >> 2) ? lane * 16 : OOB, OFF_W1 + (c * 40 + 4 * m + u) * 4096 + hw * 1024);
    }
  };
  u32 tg = 0;
  step(0, -1, -1);
  asm volatile("s_sleep 3\n\tbuffer_load_dword %0, %1, %2, %3 offen" : "+v"(tg) : "v"(0u), "s"(ws), "s"(OFF_TAG) : "memory");
  step(1, -1, -1);
  VM_BAR(9);
  __builtin_amdgcn_s_setprio(HP1);
  const u32 po = OFF_AR + r * ARENA + A_O + (10 * c + ((5 * hw) >> 1)) * 1024;
  step(2, 0, 0);
  st32<SC1>(0u, ws, (hw == 0 && lane < 11) ? lane * 32768 : OOB, id * 128);
  prefill(ws, po, 1, lane);
  VM_BAR(11); step(3, 1, 1);
  prefill(ws, po + 1024, 1, lane);
  VM_BAR(13); step(4, 2, 2);
  prefill(ws, po + 2048, 1, lane);
  VM_BAR(12); step(5, 3, 3);
  VM_BAR(9);
  PIN(tg);
  const u32 tag = __builtin_amdgcn_readfirstlane(tg) + 1u;
  st64<SC1>(u32x2{tag, ~tag}, ws, hw ? OOB : lane < 16 ? OFF_FR + (16 * r + lane) * 128 + c * 8 : lane < 32 ? OFF_FC + (16 * (lane - 16) + c) * 128 + r * 8 : OOB, 0);
  step(6, 4, 4);
  VM_BAR(11); step(7, 5, 5);
  VM_BAR(10); step(8, 6, 6);
  VM_BAR(10); step(9, 7, 7);
  VM_BAR(10); step(-1, 8, 8);
  asm volatile("s_waitcnt vmcnt(2)" ::: "memory");
  step(-1, 9, -1);
  u32 a = asu(s1[0]), b = asu((lane & 2) ? ((lane & 1) ? s2[3] : s2[2]) : ((lane & 1) ? s2[1] : s2[0]));
  swap32(a, b);
  a = asu(asf(a) + asf(b)); b = a;
  swap32(a, b);
  *(LAS u32x2 *)(uintptr_t)(L + L_STAT + hw * 256 + (lane & 31) * 8) = u32x2{a, b};
  BAR_LGKM();
  step(-1, -1, 9);
  a = asu(cs[0]); b = a;
  swap32(a, b);
  const float sum = asf(a) + asf(b);
  const u32 hi = pk(sum, sum), lo = pk(sum - asf(hi << 16), 0.f);
  const u32x4 ag = lane < 32 ? u32x4{hi, lo, 0u, 0u} : u32x4{0u, 0u, 0u, 0u};
  *(LAS u32x4 *)(uintptr_t)(L + 65536 + hw * 1024 + lane * 16) = ag;
  BAR_LGKM();
  __builtin_amdgcn_s_setprio(0);
  tag_wait(ws, ((hw & 1) ? OFF_FC : OFF_FR) + id * 128, tag, lane);
  st128<SC1>(ag, ws, lane * 16, OFF_AG + id * 12288 + hw * 1024);
  return tag;
}

__global__ __launch_bounds__(512, 2) void fused_kernel(Params p) {
  __shared__ __attribute__((aligned(1024))) char smem[L_BYTES];
  u32 L = (u32)(uintptr_t)(LAS char *)smem;
  int lane = threadIdx.x & 63;
  const int w = __builtin_amdgcn_readfirstlane(threadIdx.x >> 6);
  const int xcd = blockIdx.x & 7, bi = blockIdx.x >> 3;
  int r = MAP == 0 ? 2 * xcd + (bi >> 4) : MAP == 1 ? 4 * (xcd >> 1) + (bi >> 3) : 8 * (xcd >> 2) + (bi >> 2);
  int c = MAP == 0 ? (bi & 15) : MAP == 1 ? 8 * (xcd & 1) + (bi & 7) : 4 * (xcd & 3) + (bi & 3);
  rsrc_t ws = mkrs(p.ws, WS_USED);
  SPIN(ws);
  const bool isC = w < 4;
  const int hw = w & 3;
  u32 res[10];
  STAMPR(26); STAMP(0);
  u32 td0 = 0;
  if (isC) TOUCH(td0, lane < 5 ? (u32)(bi * 20 + hw * 5 + lane) * 128 : OOB, code_rsrc(), 0);
  if (PFA < 0 && isC) {
    __builtin_amdgcn_s_sleep(PFD);
    TOUCH(td0, lane < (w < 2 ? 38 : 40) ? lane * 128 : OOB, mkrs(w < 2 ? p.wqkv : p.x, w < 2 ? DM * 3 * DM * 2 : SEQ * DM * 2), w < 2 ? blockIdx.x * 9600 + w * 4800 : blockIdx.x * 10240 + (w & 1) * 5120);
  }
  u32 tag = 0;
#pragma unroll
  for (int j = 0; j < 10; ++j) res[j] = 0u;
  if (!isC) tag = g1_raw_helper(p, ws, L, r, c, hw, lane SPASS);
#pragma unroll 1
  for (int layer = 0; layer < 2; ++layer) {
    const int sb = 1 + layer * 7;
    (void)sb;
    u32x4 qm[2], kf[2], vf[4];
    u32x2 qt[2], tf;
    if (isC) {
      if (layer == 0) g1_compute<0>(p, ws, L, r, c, w, lane, td0 SPASS); else g1_compute<1>(p, ws, L, r, c, w, lane, td0 SPASS);
      STAMP(sb + 0);
      BAR_LGKM();
    } else if (layer) {
      FRESH();
      VM_BAR(0);
      fill_g1<1>(ws, L, c, hw, lane, 8, 10);
      dma(L + 2 * 16384 + hw * 1024, lane * 16, ws, OFF_AG + (16 * r + c) * 12288 + hw * 1024);
      VM_BAR(0);
    }
    if (!isC && layer == 0) {
      u32x4 raw[10];
      rest_load(p, r, c, hw, lane, raw);
      att_read(L, w, lane, true, qm, kf, vf, qt, tf);
      rest_store(ws, L + 49152 + hw * 7168, r, c, hw, lane, raw);
    } else {
      if (!isC) BAR();
      att_read(L, w, lane, layer == 0, qm, kf, vf, qt, tf);
    }
    STAMP(sb + 1);
    STAMP(sb + 2);
    if (layer == 1) STAMPR(24);
    attention(p.ws, ws, L, layer, r, c, w, lane, qm, kf, vf, qt, tf SPASS);
    TOUCH_DONE(td0);
    STAMP(sb + 3);
    if (layer == 1) STAMPR(25);
    if (isC) {
      if (layer == 0) {
        const rsrc_t xr = mkrs(p.x, SEQ * DM * 2);
        const int tok = 128 * r + 64 * (c & 1) + 32 * (w >> 1) + 16 * (w & 1) + (lane & 15);
#pragma unroll
        for (int j = 0; j < 5; ++j) { const u32x2 v = ld64<0>(xr, tok * (DM * 2) + (lane >> 4) * 8 + j * 32, (c >> 1) * 160); res[2 * j] = v[0]; res[2 * j + 1] = v[1]; }
      }
      group_wait(p.ws, layer * 5 + 1, 16 * r + c);
      gd_compute<10, 0, false>(p, ws, L, layer, r, c, w, lane, res SPASS);
      STAMP(sb + 4);
      group_wait(p.ws, layer * 5 + 2, 16 * r + c);
      g3_compute(p.ws, ws, L, layer, r, c, w, lane SPASS);
      STAMP(sb + 5);
      if (layer == 1) STAMPR(15);
      group_wait(p.ws, layer * 5 + 3, 16 * r + c);
      gd_compute<13, 6, true>(p, ws, L, layer, r, c, w, lane, res SPASS);
      STAMP(sb + 6);
      if (layer == 0) group_wait(p.ws, 4, 16 * r + c);
    } else {
      FRESH();
      if (layer == 0) make_aug(ws, OFF_C3 + c * 40 * 1024, 1024, OFF_AG + (16 * r + c) * 12288 + 4096, OOB, 7, 2 * hw, lane);
      BAR();
      u32x4 hold[35];
      const u32 hs = OFF_W4 + (c >> 1) * 14 * 20480 + (28 + 7 * hw) * 5120, av = lane * 16;
#pragma unroll
      for (int i = 0; i < NH0; ++i) { HLD(i); SBAR(); __builtin_amdgcn_s_sleep(1); SBAR(); }
#define VB(n) asm volatile("s_waitcnt vmcnt(%0)\n\ts_barrier" ::"i"(n) : "memory")
#define HL(a, b) _Pragma("unroll") for (int i = (a); i < (b); ++i) { HLD(i); SBAR(); if (P3) __builtin_amdgcn_s_sleep(P3); SBAR(); }
      VB(63);
      constexpr int N1 = NH0 + NHA, N2 = N1 + NHB;
      fill_g3<PACE>(ws, L, c, hw, lane, 1, 4);
      VB(14); fill_g3<P3>(ws, L, c, hw, lane, 4, 5); HL(NH0, N1);
      VB(14 + NHA); fill_g3<P3>(ws, L, c, hw, lane, 5, 6);
      dman<2, 0>(L + L_SCR + hw * 2048, lane * 16, ws, OFF_AG + (16 * r + c) * 12288 + 4096 + hw * 2048);
      VB(16 + NHA); fill_g3<P3>(ws, L, c, hw, lane, 6, 7);
      VB(16 + NHA); fill_g3<P3>(ws, L, c, hw, lane, 7, 8);
      VB(16); fill_g3<P3>(ws, L, c, hw, lane, 8, 9);
      VB(14); fill_g3<P3>(ws, L, c, hw, lane, 9, 10);
      VB(14); fill_g4<P3>(ws, L, c, hw, lane, 0, 1); HL(N1, N2);
      VB(12 + NHB); fill_g4<P3>(ws, L, c, hw, lane, 1, 2); HL(N2, 35);
      VB(10 + 35 - N1); fill_g4<P3>(ws, L, c, hw, lane, 2, 3);
      VB(10 + 35 - N1);
      fill_g4<PACE>(ws, L, c, hw, lane, 3, 7);

      group_wait(p.ws, layer * 5 + 3, 16 * r + c);
      down_helper(ws, L, layer, r, c, hw, lane, hold, hs SPASS);
    }
  }
  if (blockIdx.x == 0 && w == 4) st32<SC1>(tag, ws, lane == 0 ? 0 : OOB, OFF_TAG);
  STAMPR(27);
}

__global__ void end_of_code() {}

extern "C" void kernel_launch(void *const *d_in, const int *in_sizes, int n_in, void *d_out, int out_size, void *d_ws, size_t ws_size, hipStream_t stream) {
  Params p;
  p.x = (const unsigned short *)d_in[0];
  p.wqkv = (const unsigned short *)d_in[1];
  p.wo = (const unsigned short *)d_in[2];
  p.wg = (const unsigned short *)d_in[4];
  p.wu = (const unsigned short *)d_in[5];
  p.wd = (const unsigned short *)d_in[6];
  p.out = (unsigned short *)d_out;
  p.ws = (unsigned char *)d_ws;
  fused_kernel<<<256, 512, 0, stream>>>(p);
}
```
